```python
import torch

def _softmax(x, axis=-1):
    return torch.softmax(x, dim=axis)
def _gelu(x):
    return torch.nn.functional.gelu(x, approximate="tanh")
def _silu(x):
    return torch.nn.functional.silu(x)
def _sigmoid(x):
    return torch.sigmoid(x)
def _layernorm(x, axis=-1, eps=1e-5):
    m = x.mean(dim=axis, keepdim=True)
    v = x.var(dim=axis, keepdim=True, unbiased=False)
    return (x - m) / torch.sqrt(v + eps)
def _scatter_add(tgt, idx, src, axis=0):
    return tgt.clone().index_add_(axis, idx, src)
def _segment_sum(src, seg, n):
    out = torch.zeros((n,) + src.shape[1:], dtype=src.dtype)
    return out.index_add_(0, seg, src)
def _pad(x, pw):
    flat = tuple(v for pair in reversed(pw) for v in pair)
    return torch.nn.functional.pad(x, flat)

kernel_name = 'gated_mix__spectral_fnet__softmax_full_mha__prompt'

def setup_inputs():

    _w = (lambda a: (lambda t: t.to(torch.bfloat16) if t.is_floating_point() else t)(torch.as_tensor(a)))
    def _A_setup():
        B, S, D = 1, 2048, 1024
        ns = torch.arange(S)[:, None] * torch.arange(S)[None, :]
        nd = torch.arange(D)[:, None] * torch.arange(D)[None, :]
        Ws_re = torch.cos(-2.0 * torch.pi * ns / S).to(float)
        Ws_im = torch.sin(-2.0 * torch.pi * ns / S).to(float)
        Wd_re = torch.cos(-2.0 * torch.pi * nd / D).to(float)
        Wd_im = torch.sin(-2.0 * torch.pi * nd / D).to(float)
        return {
            "x": _w((lambda *s: torch.randn(*s, dtype=torch.bfloat16) * 0.05)(B, S, D)),
            "dft_seq_re": _w(Ws_re),
            "dft_seq_im": _w(Ws_im),
            "dft_hid_re": _w(Wd_re),
            "dft_hid_im": _w(Wd_im),
        }
    _A_in = _A_setup()

    def _B_setup():
        B, S, D = 1, 2048, 1024
        return {
            "x": _w((lambda *s: torch.randn(*s, dtype=torch.bfloat16) * 0.05)(B, S, D)),
            "wqkv": _w((lambda *s: torch.randn(*s, dtype=torch.bfloat16) * 1.6)(D, 3 * D)),
            "wo": _w((lambda *s: torch.randn(*s, dtype=torch.bfloat16) * 0.2)(D, D)),
        }
    _B_in = _B_setup()

    return {
        "A_x": _A_in["x"],
        "A_dft_seq_re": _A_in["dft_seq_re"],
        "A_dft_seq_im": _A_in["dft_seq_im"],
        "A_dft_hid_re": _A_in["dft_hid_re"],
        "A_dft_hid_im": _A_in["dft_hid_im"],
        "B_x": _B_in["x"],
        "B_wqkv": _B_in["wqkv"],
        "B_wo": _B_in["wo"],
        "gate_w": _w((lambda *s: torch.randn(*s, dtype=torch.bfloat16) * 0.05)(1024) * 16.0),
    }

def reference(A_x, A_dft_seq_re, A_dft_seq_im, A_dft_hid_re, A_dft_hid_im, B_x, B_wqkv, B_wo, gate_w):
    def _A_ref(x, dft_seq_re, dft_seq_im, dft_hid_re, dft_hid_im):
        a = torch.einsum("bsd,ed->bse", x, dft_hid_re)
        b = torch.einsum("bsd,ed->bse", x, dft_hid_im)
        y = torch.einsum("ts,bse->bte", dft_seq_re, a) - torch.einsum("ts,bse->bte", dft_seq_im, b)
        return y

    def _B_ref(x, wqkv, wo):
        B, S, D = x.shape
        H = 32; Dh = D // H
        qkv = (lambda x, *d: x.permute(*d))((x @ wqkv).reshape(B, S, 3, H, Dh), 2, 0, 3, 1, 4)
        q, k, v = qkv[0], qkv[1], qkv[2]
        scale = Dh ** -0.5
        scores = torch.einsum("bhqd,bhkd->bhqk", q, k) * scale
        p = _softmax(scores, axis=-1)
        o = torch.einsum("bhqk,bhkd->bhqd", p, v)
        return (lambda x, *d: x.permute(*d))(o, 0, 2, 1, 3).reshape(B, S, D) @ wo

    x = A_x
    g = _sigmoid((x * gate_w).sum(-1, keepdims=True))
    a = _A_ref(x, A_dft_seq_re, A_dft_seq_im, A_dft_hid_re, A_dft_hid_im)
    b = _B_ref(x, B_wqkv, B_wo)
    return g * a + (1 - g) * b
```

```cpp
#include <hip/hip_runtime.h>
#include <hip/hip_bf16.h>
typedef unsigned short u16;
__device__ inline float bf2f(u16 b) { return __uint_as_float(((unsigned)b) << 16); }
__device__ inline u16 f2bf(float f) { unsigned u = __float_as_uint(f); u += 0x7FFFu + ((u >> 16) & 1u); return (u16)(u >> 16); }
__global__ void gemm_naive(const u16* A, long am, long ak, const u16* B, long bn, long bk, float* C, int M, int N, int K, float alpha, int accum) {
    __shared__ float As[16][17], Bs[16][17];
    int tx = threadIdx.x & 15, ty = threadIdx.x >> 4;
    int m0 = blockIdx.y * 16, n0 = blockIdx.x * 16;
    float acc = 0.f;
    for (int k0 = 0; k0 < K; k0 += 16) {
        As[ty][tx] = bf2f(A[(long)(m0 + ty) * am + (long)(k0 + tx) * ak]);
        Bs[ty][tx] = bf2f(B[(long)(n0 + ty) * bn + (long)(k0 + tx) * bk]);
        __syncthreads();
        for (int k = 0; k < 16; ++k) acc += As[ty][k] * Bs[tx][k];
        __syncthreads();
    }
    long o = (long)(m0 + ty) * N + n0 + tx;
    C[o] = accum ? C[o] + alpha * acc : alpha * acc;
}
__global__ void tobf(const float* a, u16* b, long n) { long i = (long)blockIdx.x * 256 + threadIdx.x; if (i < n) b[i] = f2bf(a[i]); }
__global__ void attn_naive(const u16* qkv, u16* o) {
    int q = blockIdx.x, h = blockIdx.y; __shared__ float p[2048]; __shared__ float red[256]; __shared__ float qs[32];
    int t = threadIdx.x;
    if (t < 32) qs[t] = bf2f(qkv[(long)q * 3072 + h * 32 + t]);
    __syncthreads();
    float mx = -1e30f;
    for (int k = t; k < 2048; k += 256) { float s = 0; for (int d = 0; d < 32; ++d) s += qs[d] * bf2f(qkv[(long)k * 3072 + 1024 + h * 32 + d]); s *= 0.17677669529663687f; p[k] = s; mx = fmaxf(mx, s); }
    red[t] = mx; __syncthreads();
    for (int s = 128; s > 0; s >>= 1) { if (t < s) red[t] = fmaxf(red[t], red[t + s]); __syncthreads(); }
    mx = red[0]; __syncthreads();
    float sum = 0; for (int k = t; k < 2048; k += 256) { float e = __expf(p[k] - mx); p[k] = e; sum += e; }
    red[t] = sum; __syncthreads();
    for (int s = 128; s > 0; s >>= 1) { if (t < s) red[t] += red[t + s]; __syncthreads(); }
    sum = red[0]; __syncthreads();
    int d = t & 31, part = t >> 5; float acc = 0;
    for (int k = part; k < 2048; k += 8) acc += p[k] * bf2f(qkv[(long)k * 3072 + 2048 + h * 32 + d]);
    red[t] = acc; __syncthreads();
    if (t < 32) { float a = 0; for (int i = 0; i < 8; ++i) a += red[i * 32 + t]; o[(long)q * 1024 + h * 32 + t] = f2bf(a / sum); }
}
__global__ void combine(const u16* x, const u16* gw, const float* y, const float* b, u16* out) {
    int row = blockIdx.x, t = threadIdx.x; __shared__ float red[256];
    float s = 0; for (int d = t; d < 1024; d += 256) s += bf2f(x[(long)row * 1024 + d]) * bf2f(gw[d]);
    red[t] = s; __syncthreads();
    for (int k = 128; k > 0; k >>= 1) { if (t < k) red[t] += red[t + k]; __syncthreads(); }
    float g = 1.f / (1.f + __expf(-red[0]));
    for (int d = t; d < 1024; d += 256) out[(long)row * 1024 + d] = f2bf(g * y[(long)row * 1024 + d] + (1.f - g) * b[(long)row * 1024 + d]);
}
extern "C" void kernel_launch(void* const* d_in, const int* in_sizes, int n_in, void* d_out, int out_size, void* d_ws, size_t ws_size, hipStream_t stream) {
    const u16* x = (const u16*)d_in[0]; const u16* sre = (const u16*)d_in[1]; const u16* sim = (const u16*)d_in[2];
    const u16* hre = (const u16*)d_in[3]; const u16* him = (const u16*)d_in[4]; const u16* wqkv = (const u16*)d_in[6]; const u16* wo = (const u16*)d_in[7]; const u16* gw = (const u16*)d_in[8];
    char* ws = (char*)d_ws;
    float* tmp = (float*)ws;
    u16* a = (u16*)(ws + (32l << 20)); u16* b = (u16*)(ws + (40l << 20)); u16* qkv = (u16*)(ws + (48l << 20)); u16* o = (u16*)(ws + (64l << 20));
    float* y = (float*)(ws + (72l << 20)); float* ob = (float*)(ws + (88l << 20));
    gemm_naive<<<dim3(64, 128), 256, 0, stream>>>(x, 1024, 1, hre, 1024, 1, tmp, 2048, 1024, 1024, 1.f, 0);
    tobf<<<8192, 256, 0, stream>>>(tmp, a, 2048l * 1024);
    gemm_naive<<<dim3(64, 128), 256, 0, stream>>>(x, 1024, 1, him, 1024, 1, tmp, 2048, 1024, 1024, 1.f, 0);
    tobf<<<8192, 256, 0, stream>>>(tmp, b, 2048l * 1024);
    gemm_naive<<<dim3(64, 128), 256, 0, stream>>>(sre, 2048, 1, a, 1, 1024, y, 2048, 1024, 2048, 1.f, 0);
    gemm_naive<<<dim3(64, 128), 256, 0, stream>>>(sim, 2048, 1, b, 1, 1024, y, 2048, 1024, 2048, -1.f, 1);
    gemm_naive<<<dim3(192, 128), 256, 0, stream>>>(x, 1024, 1, wqkv, 1, 3072, tmp, 2048, 3072, 1024, 1.f, 0);
    tobf<<<24576, 256, 0, stream>>>(tmp, qkv, 2048l * 3072);
    attn_naive<<<dim3(2048, 32), 256, 0, stream>>>(qkv, o);
    gemm_naive<<<dim3(64, 128), 256, 0, stream>>>(o, 1024, 1, wo, 1, 1024, ob, 2048, 1024, 1024, 1.f, 0);
    combine<<<2048, 256, 0, stream>>>(x, gw, y, ob, (u16*)d_out);
}
```

```cpp
#include <hip/hip_runtime.h>
#include <hip/hip_bf16.h>

typedef unsigned short u16;
typedef short bf16x8 __attribute__((ext_vector_type(8)));
typedef short s16x4 __attribute__((ext_vector_type(4)));
typedef float f32x16 __attribute__((ext_vector_type(16)));
typedef float f32x4 __attribute__((ext_vector_type(4)));
typedef float f32x2 __attribute__((ext_vector_type(2)));
typedef unsigned u32x4 __attribute__((ext_vector_type(4)));
#define LAS __attribute__((address_space(3)))
typedef LAS const char* lds_cptr;

constexpr int S_LEN = 2048, D_MODEL = 1024, NHEAD = 32;
constexpr size_t WS_Q = 0;
constexpr size_t WS_KV = 4u << 20;
constexpr size_t WS_A3 = 12u << 20;
constexpr size_t WS_G = 24u << 20;

#define DIAG_BEGIN()
#define DIAG_MARK(i)
#define DIAG_END(kid, wsp)
#define MFMA32(a, b, c) __builtin_amdgcn_mfma_f32_32x32x16_bf16((a), (b), (c), 0, 0, 0)
#define MFMA16(a, b, c) __builtin_amdgcn_mfma_f32_16x16x32_bf16((a), (b), (c), 0, 0, 0)
#define SBAR() __builtin_amdgcn_sched_barrier(0)

__device__ __forceinline__ float bf2f(u16 b) { return __uint_as_float(((unsigned)b) << 16); }
__device__ __forceinline__ unsigned cvtpk(float lo, float hi) {
    typedef __bf16 bf16x2_t __attribute__((ext_vector_type(2)));
    return __builtin_bit_cast(unsigned, __builtin_convertvector((f32x2){lo, hi}, bf16x2_t)); }
__device__ __forceinline__ float bfr(float f) { return __uint_as_float(cvtpk(f, 0.f) << 16); }

__device__ __forceinline__ void glds16(unsigned voff, const void* sbase, unsigned ldsdst) {
    asm volatile("s_mov_b32 m0, %2\n\ts_nop 0\n\tglobal_load_lds_dwordx4 %0, %1" :: "v"(voff), "s"(sbase), "s"(ldsdst) : "memory");
}
template <int N> __device__ __forceinline__ void wait_bar() {
    SBAR();
    asm volatile("s_waitcnt vmcnt(%0)\n\ts_barrier" :: "n"(N) : "memory");
    SBAR();
}
__device__ __forceinline__ void lds_bar() {
    SBAR();
    asm volatile("s_waitcnt vmcnt(0) lgkmcnt(0)\n\ts_barrier" ::: "memory");
    SBAR();
}
__device__ __forceinline__ bf16x8 ld128(lds_cptr p) { return *(const LAS bf16x8*)p; }
__device__ __forceinline__ bf16x8 ldtr(lds_cptr p) {
    s16x4 lo = __builtin_amdgcn_ds_read_tr16_b64_v4i16((LAS s16x4*)p);
    s16x4 hi = __builtin_amdgcn_ds_read_tr16_b64_v4i16((LAS s16x4*)(p + 512));
    return (bf16x8){lo[0], lo[1], lo[2], lo[3], hi[0], hi[1], hi[2], hi[3]};
}
__device__ __forceinline__ u32x4 pack8(const f32x16& a, int s, float sc) {
    u32x4 w;
    w[0] = cvtpk(a[8 * s + 0] * sc, a[8 * s + 1] * sc); w[1] = cvtpk(a[8 * s + 2] * sc, a[8 * s + 3] * sc);
    w[2] = cvtpk(a[8 * s + 4] * sc, a[8 * s + 5] * sc); w[3] = cvtpk(a[8 * s + 6] * sc, a[8 * s + 7] * sc);
    return w;
}
__device__ __forceinline__ int swap23(int i) { return (i & ~12) | ((i & 4) << 1) | ((i & 8) >> 1); }

__device__ __forceinline__ void xchg_put(LAS char* p, const f32x16& a, int lane) {
#pragma unroll
    for (int i = 0; i < 4; ++i) *(LAS f32x4*)(p + i * 1024 + lane * 16) = (f32x4){a[4 * i], a[4 * i + 1], a[4 * i + 2], a[4 * i + 3]};
}
__device__ __forceinline__ void xchg_add(const LAS char* p, f32x16& a, int lane) {
#pragma unroll
    for (int i = 0; i < 4; ++i) { f32x4 v = *(const LAS f32x4*)(p + i * 1024 + lane * 16);
        a[4 * i] += v[0]; a[4 * i + 1] += v[1]; a[4 * i + 2] += v[2]; a[4 * i + 3] += v[3]; }
}

template <int MODE, int NT, int NSTAGE>
struct RT {
    static constexpr int NSEG = MODE == 0 ? 3 : 2, TN = MODE == 0 ? 3 : 2;
    static constexpr int STAGE = 16384 + NSEG * 8192, G = 2 + NSEG;
    static constexpr int LDS_BYTES = STAGE * NSTAGE;

    static __device__ __forceinline__ void run(LAS char* lds, const char* rbase, int rpitch, const char* tbase, int tpitch, int segstride,
                                               f32x16 (&acc)[2][TN]) {
        const int tid = threadIdx.x, lane = tid & 63, w = __builtin_amdgcn_readfirstlane(tid >> 6);
        const int wk = w >> 2, wm = (w >> 1) & 1, wn = w & 1;
        const int r = lane & 31, hh = lane >> 5;
        const unsigned lds0 = (unsigned)(uintptr_t)lds;
        unsigned voffR[2], voffT[NSEG];
#pragma unroll
        for (int i = 0; i < 2; ++i) { const int row = 8 * (2 * w + i) + (lane >> 3); const int c = (lane & 7) ^ ((row >> 1) & 7);
            voffR[i] = (unsigned)(row * rpitch + c * 16); }
        { const int k = 8 * w + (lane >> 3); const int c = (lane & 7) ^ (((k >> 1) & 1) << 2);
#pragma unroll
          for (int sgi = 0; sgi < NSEG; ++sgi) voffT[sgi] = (unsigned)(k * tpitch + sgi * segstride + c * 16); }
        int ra[2];
#pragma unroll
        for (int s2 = 0; s2 < 2; ++s2) ra[s2] = (64 * wm + r) * 128 + ((((2 * (2 * wk + s2)) + hh) ^ ((r >> 1) & 7)) << 4);
        const int q4 = (lane & 15) >> 2, p4 = lane & 3, cg = (lane >> 4) & 1;
        const int pi4 = ((p4 & 1) << 1) | (p4 >> 1);
        int tb[2];
#pragma unroll
        for (int sub = 0; sub < 2; ++sub) tb[sub] = 16384 + (8 * hh + q4) * 128 + ((sub ^ (q4 >> 1)) << 6) + cg * 32 + pi4 * 8 + (2 * wk) * 2048;
        const int tb0 = MODE == 0 ? (wn ? tb[1] : tb[0]) : tb[0] + wn * 8192;
        const int tb1 = tb[1] + wn * 8192;

        auto issue = [&](int kt, int st) {
            const char* rb = rbase + (size_t)kt * 128; const char* tbp = tbase + (size_t)kt * 64 * tpitch;
            const unsigned d = lds0 + st * STAGE;
#pragma unroll
            for (int i = 0; i < 2; ++i) glds16(voffR[i], rb, d + (2 * w + i) * 1024);
#pragma unroll
            for (int sgi = 0; sgi < NSEG; ++sgi) glds16(voffT[sgi], tbp, d + 16384 + sgi * 8192 + w * 1024);
        };
        bf16x8 fr0[2], ft0[TN], fr1[2], ft1[TN];
        auto rd = [&](bf16x8 (&fr)[2], bf16x8 (&ft)[TN], int st, int s2) {
            lds_cptr b = (lds_cptr)lds + st * STAGE;
#pragma unroll
            for (int i = 0; i < 2; ++i) fr[i] = ld128(b + ra[s2] + i * 4096);
#pragma unroll
            for (int tf = 0; tf < TN; ++tf) {
                if (MODE == 0) ft[tf] = ldtr(b + tb0 + s2 * 2048 + tf * 8192);
                else ft[tf] = ldtr(b + (tf ? tb1 : tb0) + s2 * 2048);
            }
        };
        auto mma = [&](bf16x8 (&fr)[2], bf16x8 (&ft)[TN]) {
#pragma unroll
            for (int i = 0; i < 2; ++i)
#pragma unroll
                for (int tf = 0; tf < TN; ++tf) {
                    if (MODE == 0 && tf == 2) acc[i][tf] = MFMA32(fr[i], ft[tf], acc[i][tf]);
                    else acc[i][tf] = MFMA32(ft[tf], fr[i], acc[i][tf]);
                }
        };
#pragma unroll
        for (int i = 0; i < 2; ++i)
#pragma unroll
            for (int tf = 0; tf < TN; ++tf) acc[i][tf] = (f32x16){};
#pragma unroll
        for (int st = 0; st < NSTAGE - 1; ++st) issue(st, st);
        wait_bar<(NSTAGE - 2) * G>();
        rd(fr0, ft0, 0, 0);
        int st = 0;
        for (int kt = 0; kt <= NT - NSTAGE; ++kt) {
            wait_bar<(NSTAGE - 3) * G>();
            const int stp = st == 0 ? NSTAGE - 1 : st - 1, stn = st == NSTAGE - 1 ? 0 : st + 1;
            issue(kt + NSTAGE - 1, stp);
            rd(fr1, ft1, st, 1);
            mma(fr0, ft0);
            rd(fr0, ft0, stn, 0);
            mma(fr1, ft1);
            st = stn;
        }
#pragma unroll
        for (int kt = NT - NSTAGE + 1; kt < NT; ++kt) {
            const int stn = st == NSTAGE - 1 ? 0 : st + 1;
            if (kt < NT - 1) {
                if (NT - 2 - kt >= NSTAGE - 3) wait_bar<(NSTAGE - 3) * G>();
                else if (NT - 2 - kt == 2) wait_bar<2 * G>();
                else if (NT - 2 - kt == 1) wait_bar<G>();
                else wait_bar<0>();
            }
            rd(fr1, ft1, st, 1);
            mma(fr0, ft0);
            if (kt < NT - 1) rd(fr0, ft0, stn, 0);
            mma(fr1, ft1);
            st = stn;
        }
        lds_bar();
        LAS char* xb = lds + (w & 3) * (2 * TN * 4096);
#pragma unroll
        for (int tf = 0; tf < TN; ++tf) { if (wk == 0) xchg_put(xb + tf * 4096, acc[1][tf], lane); else xchg_put(xb + (TN + tf) * 4096, acc[0][tf], lane); }
        lds_bar();
#pragma unroll
        for (int tf = 0; tf < TN; ++tf) {
            if (wk == 0) xchg_add(xb + (TN + tf) * 4096, acc[0][tf], lane);
            else { xchg_add(xb + tf * 4096, acc[1][tf], lane); acc[0][tf] = acc[1][tf]; }
        }
    }
};

typedef RT<0, 16, 4> RT_QKV;
__global__ __launch_bounds__(512, 2) void k_qkv(const u16* __restrict__ x, const u16* __restrict__ wqkv, const u16* __restrict__ gw, char* __restrict__ ws) {
    extern __shared__ __attribute__((aligned(16))) char smem[];
    LAS char* lds = (LAS char*)smem;
    DIAG_BEGIN();
    const int tid = threadIdx.x, lane = tid & 63, w = __builtin_amdgcn_readfirstlane(tid >> 6);
    const int xcd = blockIdx.x & 7, idx = blockIdx.x >> 3;
    const int rt = (xcd & 1) * 8 + (idx & 7), j = (xcd >> 1) * 4 + (idx >> 3);
    f32x16 acc[2][3];
    RT_QKV::run(lds, (const char*)x + (size_t)rt * 128 * 2048, 2048, (const char*)wqkv + j * 128, 6144, 2048, acc);
    const int wk = w >> 2, wm = (w >> 1) & 1, wn = w & 1;
    const int head = 2 * j + wn, t32 = rt * 4 + wm * 2 + wk;
    const float qs = 0.17677669529663687f * 1.4426950408889634f;
    char* qp = ws + WS_Q + ((size_t)(head * 64 + t32) * 2048) + lane * 16;
    char* kvp = ws + WS_KV + ((size_t)(head * 64 + t32) * 4096) + lane * 16;
#pragma unroll
    for (int s = 0; s < 2; ++s) {
        *(u32x4*)(qp + s * 1024) = pack8(acc[0][0], s, qs);
        *(u32x4*)(kvp + s * 1024) = pack8(acc[0][1], s, 1.f);
        *(u32x4*)(kvp + 2048 + s * 1024) = pack8(acc[0][2], s, 1.f);
    }
    {
        const int row = blockIdx.x * 8 + w;
        const u32x4* xr = (const u32x4*)(x + (size_t)row * 1024); const u32x4* gr = (const u32x4*)gw;
        float s = 0.f;
#pragma unroll
        for (int i = 0; i < 2; ++i) {
            const u32x4 a = xr[lane + 64 * i], b = gr[lane + 64 * i];
#pragma unroll
            for (int e = 0; e < 4; ++e) {
                s += bfr(__uint_as_float(a[e] << 16) * __uint_as_float(b[e] << 16));
                s += bfr(__uint_as_float(a[e] & 0xffff0000u) * __uint_as_float(b[e] & 0xffff0000u));
            }
        }
#pragma unroll
        for (int o = 32; o >= 1; o >>= 1) s += __shfl_xor(s, o);
        const float z = bfr(s);
        const float g = bfr(1.f / (1.f + __expf(-z)));
        if (lane == 0) *(f32x2*)(ws + WS_G + (size_t)row * 8) = (f32x2){g, bfr(1.f - g)};
    }
    DIAG_END(0, ws);
}

typedef RT<1, 32, 4> RT_UW;
__global__ __launch_bounds__(512, 2) void k_uw(const u16* __restrict__ x, const u16* __restrict__ cr, const u16* __restrict__ ci, char* __restrict__ ws) {
    extern __shared__ __attribute__((aligned(16))) char smem[];
    LAS char* lds = (LAS char*)smem;
    DIAG_BEGIN();
    const int tid = threadIdx.x, lane = tid & 63, w = __builtin_amdgcn_readfirstlane(tid >> 6);
    const int xcd = blockIdx.x & 7, idx = blockIdx.x >> 3;
    const int tt = (xcd >> 1) * 8 + (idx & 7), dt = (xcd & 1) * 4 + (idx >> 3);
    const int part = tt >> 4, trow = (tt & 15) * 128;
    const u16* cm = part ? ci : cr;
    f32x16 acc[2][2];
    RT_UW::run(lds, (const char*)cm + (size_t)trow * 4096, 4096, (const char*)x + dt * 256, 2048, 128, acc);
    const int wk = w >> 2, wm = (w >> 1) & 1, wn = w & 1;
    const int t32 = (tt & 15) * 4 + wm * 2 + wk;
    float g = *(const float*)(ws + WS_G + (size_t)(t32 * 32 + (lane & 31)) * 8);
    if (part) g = -g;
    char* ap = ws + WS_A3 + ((size_t)t32 * 192 + part * 64 + dt * 8 + wn * 4) * 1024 + lane * 16;
#pragma unroll
    for (int tf = 0; tf < 2; ++tf)
#pragma unroll
        for (int s = 0; s < 2; ++s) *(u32x4*)(ap + (tf * 2 + s) * 1024) = pack8(acc[0][tf], s, g);
    DIAG_END(1, ws);
}

constexpr int AT_CH = 4, AT_STAGE = AT_CH * 4096, AT_NST = 4, AT_NCH = 64 / AT_CH;
__global__ __launch_bounds__(512, 2) void k_attn(char* __restrict__ ws) {
    extern __shared__ __attribute__((aligned(16))) char smem[];
    LAS char* lds = (LAS char*)smem;
    DIAG_BEGIN();
    const int tid = threadIdx.x, lane = tid & 63, w = __builtin_amdgcn_readfirstlane(tid >> 6);
    const int xcd = blockIdx.x & 7, idx = blockIdx.x >> 3;
    const int head = xcd * 4 + (idx >> 3), qt = (idx & 7) * 8 + w;
    const char* kvb = ws + WS_KV + (size_t)head * 64 * 4096;
    const unsigned lds0 = (unsigned)(uintptr_t)lds;
    const unsigned voff = (unsigned)(w * 2048 + lane * 16);
    auto issue = [&](int c, int st) {
        const char* b = kvb + (size_t)c * AT_STAGE;
        glds16(voff, b, lds0 + st * AT_STAGE + w * 2048);
        glds16(voff, b + 1024, lds0 + st * AT_STAGE + w * 2048 + 1024);
    };
    bf16x8 qf[2];
    { const char* qp = ws + WS_Q + (size_t)(head * 64 + qt) * 2048 + lane * 16;
      qf[0] = *(const bf16x8*)qp; qf[1] = *(const bf16x8*)(qp + 1024); }
    const float omg = *(const float*)(ws + WS_G + (size_t)(qt * 32 + (lane & 31)) * 8 + 4);
    asm volatile("" :: "v"(qf[0]), "v"(qf[1]), "v"(omg));
#pragma unroll
    for (int st = 0; st < AT_NST - 1; ++st) issue(st, st);
    bf16x8 sel; { const short one = (((lane & 1) == ((lane >> 4) & 1))) ? (short)0x3f80 : (short)0;
#pragma unroll
        for (int i = 0; i < 8; ++i) sel[i] = one; }
    f32x16 o = (f32x16){}; f32x4 l4 = (f32x4){};
    f32x16 negm, sA, sB;
    bf16x8 kf[2], vf[2];
    lds_cptr lp = (lds_cptr)lds + lane * 16;
    wait_bar<(AT_NST - 2) * 2>();
    kf[0] = ld128(lp); kf[1] = ld128(lp + 1024);
    sA = MFMA32(kf[0], qf[0], (f32x16){}); sA = MFMA32(kf[1], qf[1], sA);
    { float m = sA[0];
#pragma unroll
      for (int i = 1; i < 16; ++i) m = fmaxf(m, sA[i]);
      m = fmaxf(m, __shfl_xor(m, 32));
#pragma unroll
      for (int i = 0; i < 16; ++i) { negm[i] = -m; sA[i] -= m; } }
    kf[0] = ld128(lp + 4096); kf[1] = ld128(lp + 4096 + 1024);

#define ATT_STEP(SC, SN, vo, kn, DO_QK, DO_KLD) do { \
        vf[0] = ld128(lp + (vo)); vf[1] = ld128(lp + (vo) + 1024); \
        if (DO_QK) { SN = MFMA32(kf[0], qf[0], negm); SN = MFMA32(kf[1], qf[1], SN); } \
        if (DO_KLD) { kf[0] = ld128(lp + (kn)); kf[1] = ld128(lp + (kn) + 1024); } \
        u32x4 p0, p1; \
        _Pragma("unroll") for (int i = 0; i < 16; ++i) SC[i] = __builtin_amdgcn_exp2f(SC[i]); \
        _Pragma("unroll") for (int i = 0; i < 4; ++i) { p0[i] = cvtpk(SC[2 * i], SC[2 * i + 1]); p1[i] = cvtpk(SC[8 + 2 * i], SC[9 + 2 * i]); } \
        const bf16x8 pb0 = __builtin_bit_cast(bf16x8, p0), pb1 = __builtin_bit_cast(bf16x8, p1); \
        o = MFMA32(vf[0], pb0, o); l4 = MFMA16(sel, pb0, l4); \
        o = MFMA32(vf[1], pb1, o); l4 = MFMA16(sel, pb1, l4); \
    } while (0)

    int st = 0;
    for (int c = 0; c < AT_NCH; ++c) {
        const int stn = st == AT_NST - 1 ? 0 : st + 1, stp = st == 0 ? AT_NST - 1 : st - 1;
        if (c < AT_NCH - 2) wait_bar<(AT_NST - 3) * 2>(); else wait_bar<0>();
        if (c + AT_NST - 1 < AT_NCH) issue(c + AT_NST - 1, stp);
        const int b0 = st * AT_STAGE, b1 = stn * AT_STAGE;
        const bool more = c + 1 < AT_NCH;
        ATT_STEP(sA, sB, b0 + 2048, b0 + 2 * 4096, true, true);
        ATT_STEP(sB, sA, b0 + 4096 + 2048, b0 + 3 * 4096, true, true);
        ATT_STEP(sA, sB, b0 + 2 * 4096 + 2048, b1, true, true);
        ATT_STEP(sB, sA, b0 + 3 * 4096 + 2048, b1 + 4096, more, true);
        st = stn;
    }
#undef ATT_STEP
    float l = ((lane >> 4) & 1) ? l4[1] : l4[0];
    bool ok = l < 1.0e30f;
#pragma unroll
    for (int i = 0; i < 16; ++i) ok = ok && (fabsf(o[i]) < 3.0e38f);
    if (!__all(ok)) {
        float m = -INFINITY, ls = 0.f; o = (f32x16){};
        const char* gp = kvb + lane * 16;
        for (int t = 0; t < 64; ++t) {
            const bf16x8 k0 = *(const bf16x8*)(gp + t * 4096), k1 = *(const bf16x8*)(gp + t * 4096 + 1024);
            const bf16x8 v0 = *(const bf16x8*)(gp + t * 4096 + 2048), v1 = *(const bf16x8*)(gp + t * 4096 + 3072);
            f32x16 s = MFMA32(k0, qf[0], (f32x16){}); s = MFMA32(k1, qf[1], s);
            float tm = s[0];
#pragma unroll
            for (int i = 1; i < 16; ++i) tm = fmaxf(tm, s[i]);
            tm = fmaxf(tm, __shfl_xor(tm, 32));
            const float mn = fmaxf(m, tm), al = __builtin_amdgcn_exp2f(m - mn);
            m = mn; ls *= al;
#pragma unroll
            for (int i = 0; i < 16; ++i) { o[i] *= al; s[i] = __builtin_amdgcn_exp2f(s[i] - mn); ls += s[i]; }
            u32x4 p0, p1;
#pragma unroll
            for (int i = 0; i < 4; ++i) { p0[i] = cvtpk(s[2 * i], s[2 * i + 1]); p1[i] = cvtpk(s[8 + 2 * i], s[9 + 2 * i]); }
            o = MFMA32(v0, __builtin_bit_cast(bf16x8, p0), o); o = MFMA32(v1, __builtin_bit_cast(bf16x8, p1), o);
        }
        l = ls + __shfl_xor(ls, 32);
    }
    const float sc = omg / l;
    char* ap = ws + WS_A3 + ((size_t)qt * 192 + 128 + 2 * head) * 1024 + lane * 16;
#pragma unroll
    for (int s = 0; s < 2; ++s) *(u32x4*)(ap + s * 1024) = pack8(o, s, sc);
    DIAG_END(2, ws);
}

constexpr int F_STAGE = 24576, F_NST = 6, F_NT = 48, F_G = 3;
__global__ __launch_bounds__(512, 2) void k_final(const char* __restrict__ ws, const u16* __restrict__ hr, const u16* __restrict__ hi,
                                                   const u16* __restrict__ wo, u16* __restrict__ out) {
    extern __shared__ __attribute__((aligned(16))) char smem[];
    LAS char* lds = (LAS char*)smem;
    DIAG_BEGIN();
    const int tid = threadIdx.x, lane = tid & 63, w = __builtin_amdgcn_readfirstlane(tid >> 6);
    const int xcd = blockIdx.x & 7, idx = blockIdx.x >> 3;
    const int tt = (xcd >> 1) * 4 + (idx & 3), et = (xcd & 1) * 8 + (idx >> 2);
    const int wk = w >> 2, wm = (w >> 1) & 1, wn = w & 1;
    const int r = lane & 31, hh = lane >> 5;
    const unsigned lds0 = (unsigned)(uintptr_t)lds;
    const unsigned voffA = (unsigned)(((w >> 1) * 192 + 2 * (w & 1)) * 1024 + lane * 16);
    unsigned voffBR, voffBT;
    { const int row = 8 * w + (lane >> 3);
      voffBR = (unsigned)(row * 2048 + (((lane & 7) ^ ((row >> 1) & 7)) << 4));
      voffBT = (unsigned)(row * 2048 + (((lane & 7) ^ (((row >> 1) & 1) << 2)) << 4)); }
    const char* abase = ws + WS_A3 + (size_t)tt * 4 * 192 * 1024;
    const char* b0 = (const char*)hr + (size_t)et * 64 * 2048;
    const char* b1 = (const char*)hi + (size_t)et * 64 * 2048;
    const char* b2 = (const char*)wo + et * 128;
    auto issue = [&](int kt, int st) {
        const unsigned d = lds0 + st * F_STAGE;
        const char* ab = abase + (size_t)kt * 4096;
        glds16(voffA, ab, d + (2 * w) * 1024);
        glds16(voffA, ab + 1024, d + (2 * w + 1) * 1024);
        if (kt < 32) { const char* bb = (kt < 16 ? b0 : b1) + (kt & 15) * 128; glds16(voffBR, bb, d + 16384 + w * 1024); }
        else glds16(voffBT, b2 + (size_t)(kt - 32) * 64 * 2048, d + 16384 + w * 1024);
    };
    const int aoff = (wm * 2 * 4 + 2 * wk) * 1024 + lane * 16;
    int rb[2];
    { const int er = 32 * wn + swap23(r);
#pragma unroll
      for (int s2 = 0; s2 < 2; ++s2) rb[s2] = 16384 + er * 128 + ((((2 * (2 * wk + s2)) + hh) ^ ((er >> 1) & 7)) << 4); }
    const int q4 = (lane & 15) >> 2, p4 = lane & 3, cg = (lane >> 4) & 1, pi4 = ((p4 & 1) << 1) | (p4 >> 1);
    const int tb = 16384 + (8 * hh + q4) * 128 + ((wn ^ (q4 >> 1)) << 6) + cg * 32 + pi4 * 8 + (2 * wk) * 2048;
    f32x16 acc[2]; acc[0] = (f32x16){}; acc[1] = (f32x16){};
    bf16x8 fa0[2], fb0, fa1[2], fb1;
    auto rd = [&](bf16x8 (&fa)[2], bf16x8& fb, int st, int s2, bool isT) {
        lds_cptr b = (lds_cptr)lds + st * F_STAGE;
        fa[0] = ld128(b + aoff + s2 * 1024); fa[1] = ld128(b + aoff + s2 * 1024 + 4096);
        if (isT) fb = ldtr(b + tb + s2 * 2048); else fb = ld128(b + rb[s2]);
    };
    auto mma = [&](bf16x8 (&fa)[2], bf16x8& fb) { acc[0] = MFMA32(fb, fa[0], acc[0]); acc[1] = MFMA32(fb, fa[1], acc[1]); };
#pragma unroll
    for (int st = 0; st < F_NST - 1; ++st) issue(st, st);
    wait_bar<(F_NST - 2) * F_G>();
    rd(fa0, fb0, 0, 0, false);
    int st = 0;
#define F_ITER(kt, WAIT, DO_ISSUE, CUR_T, NXT_T, DO_NEXT) do { \
        const int stp = st == 0 ? F_NST - 1 : st - 1, stn = st == F_NST - 1 ? 0 : st + 1; \
        WAIT; \
        if (DO_ISSUE) issue((kt) + F_NST - 1, stp); \
        rd(fa1, fb1, st, 1, CUR_T); mma(fa0, fb0); \
        if (DO_NEXT) rd(fa0, fb0, stn, 0, NXT_T); \
        mma(fa1, fb1); st = stn; } while (0)
    for (int kt = 0; kt < 31; ++kt) F_ITER(kt, wait_bar<(F_NST - 3) * F_G>(), true, false, false, true);
    F_ITER(31, wait_bar<(F_NST - 3) * F_G>(), true, false, true, true);
    for (int kt = 32; kt <= F_NT - F_NST; ++kt) F_ITER(kt, wait_bar<(F_NST - 3) * F_G>(), true, true, true, true);
    F_ITER(43, wait_bar<3 * F_G>(), false, true, true, true);
    F_ITER(44, wait_bar<2 * F_G>(), false, true, true, true);
    F_ITER(45, wait_bar<1 * F_G>(), false, true, true, true);
    F_ITER(46, wait_bar<0>(), false, true, true, true);
    F_ITER(47, (void)0, false, true, true, false);
#undef F_ITER
    static_assert(F_NST == 6, "tail written for 6 stages");
    lds_bar();
    LAS char* xb = lds + (w & 3) * 8192;
    if (wk == 0) xchg_put(xb, acc[1], lane); else xchg_put(xb + 4096, acc[0], lane);
    lds_bar();
    if (wk == 0) xchg_add(xb + 4096, acc[0], lane); else { xchg_add(xb, acc[1], lane); acc[0] = acc[1]; }
    const int row = tt * 128 + wm * 64 + wk * 32 + r;
    u16* op = out + (size_t)row * 1024 + et * 64 + wn * 32 + hh * 8;
#pragma unroll
    for (int s = 0; s < 2; ++s) *(u32x4*)(op + s * 16) = pack8(acc[0], s, 1.f);
    DIAG_END(3, ws);
}

extern "C" void kernel_launch(void* const* d_in, const int* in_sizes, int n_in, void* d_out, int out_size, void* d_ws, size_t ws_size,
                              hipStream_t stream) {
    const u16* x = (const u16*)d_in[0]; const u16* cr = (const u16*)d_in[1]; const u16* ci = (const u16*)d_in[2];
    const u16* hr = (const u16*)d_in[3]; const u16* hi = (const u16*)d_in[4];
    const u16* wqkv = (const u16*)d_in[6]; const u16* wo = (const u16*)d_in[7]; const u16* gw = (const u16*)d_in[8];
    char* ws = (char*)d_ws;
    (void)hipFuncSetAttribute((const void*)k_qkv, hipFuncAttributeMaxDynamicSharedMemorySize, RT_QKV::LDS_BYTES);
    (void)hipFuncSetAttribute((const void*)k_uw, hipFuncAttributeMaxDynamicSharedMemorySize, RT_UW::LDS_BYTES);
    (void)hipFuncSetAttribute((const void*)k_attn, hipFuncAttributeMaxDynamicSharedMemorySize, AT_STAGE * AT_NST);
    (void)hipFuncSetAttribute((const void*)k_final, hipFuncAttributeMaxDynamicSharedMemorySize, F_STAGE * F_NST);
    k_qkv<<<256, 512, RT_QKV::LDS_BYTES, stream>>>(x, wqkv, gw, ws);
    k_uw<<<256, 512, RT_UW::LDS_BYTES, stream>>>(x, cr, ci, ws);
    k_attn<<<256, 512, AT_STAGE * AT_NST, stream>>>(ws);
    k_final<<<256, 512, F_STAGE * F_NST, stream>>>(ws, hr, hi, wo, (u16*)d_out);
}
```

```cpp
#include <hip/hip_runtime.h>
#include <hip/hip_bf16.h>

typedef unsigned short u16;
typedef short bf16x8 __attribute__((ext_vector_type(8)));
typedef short s16x4 __attribute__((ext_vector_type(4)));
typedef float f32x16 __attribute__((ext_vector_type(16)));
typedef float f32x4 __attribute__((ext_vector_type(4)));
typedef float f32x2 __attribute__((ext_vector_type(2)));
typedef unsigned u32x4 __attribute__((ext_vector_type(4)));
#define LAS __attribute__((address_space(3)))
typedef LAS const char* lds_cptr;

constexpr size_t WS_Q = 0;
constexpr size_t WS_KV = 4u << 20;
constexpr size_t WS_AB = 12u << 20;
constexpr size_t WS_O = 20u << 20;
constexpr size_t WS_Y = 24u << 20;
constexpr size_t WS_G = 40u << 20;

#define DIAG_BEGIN()
#define DIAG_MARK(i)
#define DIAG_END(kid, wsp)

#define MFMA32(a, b, c) __builtin_amdgcn_mfma_f32_32x32x16_bf16((a), (b), (c), 0, 0, 0)
#define MFMA16(a, b, c) __builtin_amdgcn_mfma_f32_16x16x32_bf16((a), (b), (c), 0, 0, 0)
#define SBAR() __builtin_amdgcn_sched_barrier(0)
#define PIN(x) asm volatile("" : "+v"(x))
#define EX2(v) __builtin_amdgcn_exp2f(v)
#define PB(p) __builtin_bit_cast(bf16x8, p)
#define LGKM0() __builtin_amdgcn_s_waitcnt(0xC07F)

__device__ __forceinline__ unsigned cvtpk(float lo, float hi) {
    typedef __bf16 bf16x2_t __attribute__((ext_vector_type(2)));
    return __builtin_bit_cast(unsigned, __builtin_convertvector((f32x2){lo, hi}, bf16x2_t)); }
__device__ __forceinline__ float bfr(float f) { return __uint_as_float(cvtpk(f, 0.f) << 16); }

__device__ __forceinline__ void glds16(unsigned voff, const void* sbase, unsigned ldsdst) {
    asm volatile("s_mov_b32 m0, %2\n\ts_nop 0\n\tglobal_load_lds_dwordx4 %0, %1" :: "v"(voff), "s"(sbase), "s"(ldsdst) : "memory");
}
template <int N> __device__ __forceinline__ void wait_bar() {
    SBAR();
    asm volatile("s_waitcnt vmcnt(%0)\n\ts_barrier" :: "n"(N) : "memory");
    SBAR();
}
__device__ __forceinline__ void bar() { SBAR(); asm volatile("s_barrier" ::: "memory"); SBAR(); }
__device__ __forceinline__ void bar_l1() {
    SBAR();
    asm volatile("s_waitcnt lgkmcnt(1)\n\ts_barrier" ::: "memory");
    SBAR();
}
__device__ __forceinline__ bf16x8 ld128(lds_cptr p) { return *(const LAS bf16x8*)p; }
__device__ __forceinline__ bf16x8 ldtr(lds_cptr p) {
    s16x4 lo = __builtin_amdgcn_ds_read_tr16_b64_v4i16((LAS s16x4*)p);
    s16x4 hi = __builtin_amdgcn_ds_read_tr16_b64_v4i16((LAS s16x4*)(p + 512));
    return (bf16x8){lo[0], lo[1], lo[2], lo[3], hi[0], hi[1], hi[2], hi[3]};
}
__device__ __forceinline__ u32x4 pack8(const f32x16& a, int s, float sc) {
    u32x4 w;
    w[0] = cvtpk(a[8 * s + 0] * sc, a[8 * s + 1] * sc); w[1] = cvtpk(a[8 * s + 2] * sc, a[8 * s + 3] * sc);
    w[2] = cvtpk(a[8 * s + 4] * sc, a[8 * s + 5] * sc); w[3] = cvtpk(a[8 * s + 6] * sc, a[8 * s + 7] * sc);
    return w;
}
#define ST16(p, v) do { const u32x4 v_ = (v); asm volatile("global_store_dwordx4 %0, %1, off sc0 sc1\n\ts_nop 1" :: "v"(p), "v"(v_) : "memory"); } while (0)
__device__ __forceinline__ int swap23(int i) { return (i & ~12) | ((i & 4) << 1) | ((i & 8) >> 1); }

__device__ __forceinline__ int lf_order(int tau, int a, int c, int G) {
    if (tau == 0) return a;
    const int g = (tau - 1) >> 2, pos = (tau - 1) & 3;
    if (g != G - 1 && pos == c) return 4 * (g + 1) + a;
    const int b = (g == G - 1 || pos < c) ? pos : pos - 1;
    return 4 * g + (b < a ? b : b + 1);
}
#define NX3(v) ((v) == 2 ? 0 : (v) + 1)
#define NX5(v) ((v) == 4 ? 0 : (v) + 1)

constexpr int P_RS = 32768, P_TS = 12288, P_TB = 3 * P_RS, P_GW = P_TB + 5 * P_TS, P_LDS = P_GW + 2048;
__device__ __forceinline__ int rswz(int row) { return ((row >> 1) & 1) | (((row >> 3) & 3) << 1); }
__device__ __forceinline__ u32x4 pack44(const f32x4& a, const f32x4& b, float sc) {
    return (u32x4){cvtpk(a[0] * sc, a[1] * sc), cvtpk(a[2] * sc, a[3] * sc), cvtpk(b[0] * sc, b[1] * sc), cvtpk(b[2] * sc, b[3] * sc)};
}
__global__ __launch_bounds__(512, 2) void k_proj(const u16* __restrict__ x, const u16* __restrict__ wqkv, const u16* __restrict__ hr,
                                                  const u16* __restrict__ hi, const u16* __restrict__ gw, char* __restrict__ ws) {
    extern __shared__ __attribute__((aligned(16))) char smem[];
    LAS char* lds = (LAS char*)smem;
    DIAG_BEGIN();
    const int tid = threadIdx.x, lane = tid & 63, w = __builtin_amdgcn_readfirstlane(tid >> 6);
    const int xcd = blockIdx.x & 7, idx = blockIdx.x >> 3;
    const int rt = (xcd & 1) * 8 + (idx & 7), j = (xcd >> 1) * 4 + (idx >> 3);
    if (w >= 4) {
        const int g = w - 4;
        const int la = ((idx >> 3) - (idx & 7)) & 3, lc = (idx >> 1) & 3;
        const int kptab = lf_order(lane & 15, la, lc, 4);
        const unsigned lds0 = (unsigned)(uintptr_t)lds;
        const char* xb = (const char*)x + (size_t)(rt * 128 + g * 32) * 2048;
        const char* hb = (const char*)(j < 8 ? hr : hi) + (size_t)((j & 7) * 128 + g * 32) * 2048;
        const char* wb = (const char*)wqkv + (size_t)g * 8 * 6144 + j * 128;
        unsigned vR[4];
#pragma unroll
        for (int i = 0; i < 4; ++i) vR[i] = (unsigned)((8 * i + (lane >> 3)) * 2048 + (((lane & 7) ^ rswz(8 * i + (lane >> 3))) << 4));
        const unsigned vT = (unsigned)((lane >> 3) * 6144 + (((lane & 7) ^ (((lane >> 4) & 1) << 2) ^ ((g & 1) << 1)) << 4));
        auto dT = [&](int kp, int hf, int ts) {
            const char* b = wb + (size_t)(kp * 64 + hf * 32) * 6144; const unsigned d = lds0 + P_TB + ts * P_TS + g * 1024;
            glds16(vT, b, d); glds16(vT, b + 2048, d + 4096); glds16(vT, b + 4096, d + 8192);
        };
        auto dR = [&](int kp, int hf, int rs) {
            const char* b = (hf ? hb : xb) + kp * 128; const unsigned d = lds0 + rs * P_RS + hf * 16384 + g * 4096;
#pragma unroll
            for (int i = 0; i < 4; ++i) glds16(vR[i], b, d + i * 1024);
        };
        const int grow = 8 * j + 2 * g + ((lane >> 3) & 1);
        lds_cptr gxa = (lds_cptr)lds + grow * 128 + (((lane & 7) ^ rswz(grow)) << 4);
        lds_cptr gwa = (lds_cptr)lds + P_GW + (lane & 7) * 16;
        float z = 0.f;
        auto gate = [&](int kp, int rs) {
            const u32x4 a = *(const LAS u32x4*)(gxa + rs * P_RS), b = *(const LAS u32x4*)(gwa + kp * 128);
#pragma unroll
            for (int e = 0; e < 4; ++e) {
                z = fmaf(__uint_as_float(a[e] << 16), __uint_as_float(b[e] << 16), z);
                z = fmaf(__uint_as_float(a[e] & 0xffff0000u), __uint_as_float(b[e] & 0xffff0000u), z);
            }
        };
        { const int k0 = __builtin_amdgcn_readlane(kptab, 0), k1 = __builtin_amdgcn_readlane(kptab, 1), k2 = __builtin_amdgcn_readlane(kptab, 2);
          dR(k0, 0, 0); dT(k0, 0, 0); dR(k0, 1, 0);
          glds16((unsigned)(lane * 16), (const char*)gw + (g & 1) * 1024, lds0 + P_GW + (g & 1) * 1024);
          dT(k0, 1, 1);
          wait_bar<3>();
          dT(k1, 0, 2); dR(k1, 0, 1);
          wait_bar<7>();
          dT(k1, 1, 3); dR(k1, 1, 1); dT(k2, 0, 4); dR(k2, 0, 2);
          gate(k0, 0); }
        int rsp = 2, tsp = 0;
        for (int kt = 1; kt < 14; ++kt) {
            const int kp0 = __builtin_amdgcn_readlane(kptab, kt), kp1 = __builtin_amdgcn_readlane(kptab, kt + 1), kp2 = __builtin_amdgcn_readlane(kptab, kt + 2);
            wait_bar<7>();
            dT(kp1, 1, tsp); dR(kp1, 1, rsp); tsp = NX5(tsp); rsp = NX3(rsp);
            gate(kp0, NX3(rsp));
            wait_bar<18>();
            dT(kp2, 0, tsp); dR(kp2, 0, rsp); tsp = NX5(tsp);
        }
        { const int kp0 = __builtin_amdgcn_readlane(kptab, 14), kp1 = __builtin_amdgcn_readlane(kptab, 15);
          wait_bar<7>();
          dT(kp1, 1, tsp); dR(kp1, 1, rsp);
          gate(kp0, 2);
          wait_bar<18>();
          wait_bar<0>();
          gate(kp1, 0);
          bar(); }
        z += __shfl_xor(z, 1); z += __shfl_xor(z, 2); z += __shfl_xor(z, 4);
        const float gt = bfr(1.f / (1.f + __expf(-bfr(z))));
        if ((lane & 55) == 0) *(f32x2*)(ws + WS_G + (size_t)(rt * 128 + grow) * 8) = (f32x2){gt, bfr(1.f - gt)};
    } else {
        const int wm = w >> 1, wn = w & 1, c = lane & 15, g4 = lane >> 4;
        const int sg0 = 8 * (c >> 2) + (c & 3);
        int ra[2], tb[2];
#pragma unroll
        for (int hf = 0; hf < 2; ++hf) ra[hf] = sg0 * 128 + (((4 * hf + g4) ^ rswz(sg0)) << 4);
        { const int q4 = c >> 2, p4 = c & 3;
#pragma unroll
          for (int dt = 0; dt < 2; ++dt)
              tb[dt] = P_TB + (8 * g4 + q4) * 128 + ((((wn * 4 + dt * 2) ^ ((q4 >> 1) << 2) ^ ((g4 & 1) << 1)) + (p4 >> 1)) << 4) + (p4 & 1) * 8; }
        const int xoff = wm * 8192, hoff = 16384 + wn * 8192;
        f32x4 acc[10][4];
#pragma unroll
        for (int b = 0; b < 10; ++b)
#pragma unroll
            for (int m = 0; m < 4; ++m) acc[b][m] = (f32x4){};
        bf16x8 xA[4], xB[4], fb[5];
#pragma unroll
        for (int m = 0; m < 4; ++m) xB[m] = (bf16x8){};
#pragma unroll
        for (int b = 0; b < 5; ++b) fb[b] = (bf16x8){};
        auto rdB = [&](int b, lds_cptr rb, lds_cptr t0, lds_cptr t1) {
            if (b < 6) fb[b % 5] = ldtr(((b & 1) ? t1 : t0) + (b >> 1) * 4096);
            else fb[b % 5] = ld128(rb + hoff + ((b - 6) >> 1) * 4096 + (b & 1) * 512);
        };
        auto mm = [&](int b, bf16x8 (&xf)[4], int m) {
            if (b < 4) acc[b][m] = MFMA16(fb[b % 5], xf[m], acc[b][m]);
            else acc[b][m] = MFMA16(xf[m], fb[b % 5], acc[b][m]);
        };
#define P_HALF(XP, XC, RS, TS, HF) do { \
            lds_cptr rb_ = (lds_cptr)lds + (RS) * P_RS + ra[HF]; \
            lds_cptr t0_ = (lds_cptr)lds + (TS) * P_TS + tb[0]; lds_cptr t1_ = (lds_cptr)lds + (TS) * P_TS + tb[1]; \
            bar(); \
            _Pragma("unroll") for (int s_ = 0; s_ < 10; ++s_) { \
                _Pragma("unroll") for (int m = 0; m < 4; ++m) { \
                    if (s_ < 3) mm(s_ + 7, XP, m); else mm(s_ - 3, XC, m); \
                    if (s_ == 0) XC[m] = ld128(rb_ + xoff + (m >> 1) * 4096 + (m & 1) * 512); \
                    if (m == 3) rdB(s_, rb_, t0_, t1_); \
                    SBAR(); } } } while (0)
        int rs = 0, ts = 0;
        LGKM0();
        for (int kt = 0; kt < 16; ++kt) {
            const int ts1 = NX5(ts);
            P_HALF(xB, xA, rs, ts, 0);
            P_HALF(xA, xB, rs, ts1, 1);
            ts = NX5(ts1); rs = NX3(rs);
        }
#pragma unroll
        for (int b = 7; b < 10; ++b)
#pragma unroll
            for (int m = 0; m < 4; ++m) mm(b, xB, m);
#undef P_HALF
        DIAG_MARK(1);
        const int head = 2 * j + wn;
        const float qs = 0.17677669529663687f * 1.4426950408889634f;
        const int fo = (g4 >> 1) * 1024 + (g4 & 1) * 512;
#pragma unroll
        for (int gr = 0; gr < 2; ++gr) {
            const int t32 = rt * 4 + wm * 2 + gr;
            char* qp = ws + WS_Q + (size_t)(head * 64 + t32) * 2048 + fo;
            char* kp = ws + WS_KV + (size_t)(head * 64 + t32) * 2048 + fo;
#pragma unroll
            for (int a = 0; a < 2; ++a) {
                ST16(qp + (sg0 + 4 * a) * 16, pack44(acc[0][2 * gr + a], acc[1][2 * gr + a], qs));
                ST16(kp + (16 * (c >> 3) + 8 * a + (c & 7)) * 16, pack44(acc[2][2 * gr + a], acc[3][2 * gr + a], 1.f));
            }
#pragma unroll
            for (int dt = 0; dt < 2; ++dt)
                ST16(kp + (4u << 20) + (16 * dt + swap23(c)) * 16, pack44(acc[4 + dt][2 * gr], acc[4 + dt][2 * gr + 1], 1.f));
            char* abp = ws + WS_AB + ((size_t)(j * 8 + wn * 4) * 64 + t32) * 1024 + lane * 16;
#pragma unroll
            for (int b = 0; b < 4; ++b) ST16(abp + (size_t)b * 65536, pack44(acc[6 + b][2 * gr], acc[6 + b][2 * gr + 1], 1.f));
        }
    }
    DIAG_END(0, ws);
}

constexpr int M_NST = 4, M_KS = 8192, M_G0 = M_NST * M_KS, M_GS = 32768, M_AB = 16384, M_LDS = M_G0 + M_NST * M_GS;
__global__ __launch_bounds__(1024, 4) void k_mid(const u16* __restrict__ cr, const u16* __restrict__ ci, const u16* __restrict__ wo, char* __restrict__ ws) {
    extern __shared__ __attribute__((aligned(16))) char smem[];
    LAS char* lds = (LAS char*)smem;
    DIAG_BEGIN();
    const int tid = threadIdx.x, lane = tid & 63, w = __builtin_amdgcn_readfirstlane(tid >> 6);
    const int xcd = blockIdx.x & 7, idx = blockIdx.x >> 3;
    const int head = xcd * 4 + (idx >> 3);
    const int part = xcd >> 2, tt = ((xcd >> 1) & 1) * 8 + (idx & 7), et = (xcd & 1) * 4 + (idx >> 3);
    const int r = lane & 31, hh = lane >> 5;
    const char* kvb = ws + WS_KV + (size_t)head * 64 * 2048;
    lds_cptr lp = (lds_cptr)lds + lane * 16;
    if (w >= 12) {
        const int g = w - 12;
        const int la = ((idx >> 3) - (idx & 7)) & 3, lc = (idx >> 1) & 3;
        const int kptab = lf_order(lane & 31, la, lc, 8);
        const unsigned lds0 = (unsigned)(uintptr_t)lds;
        const char* cb = (const char*)(part ? ci : cr) + (size_t)(tt * 128 + 32 * g) * 4096;
        const char* ab = ws + WS_AB + (size_t)(part * 64 + et * 8 + 2 * g) * 65536;
        const unsigned vC0 = (unsigned)((lane >> 3) * 4096 + (((lane & 7) ^ ((lane >> 4) & 7)) << 4));
        const unsigned vC1 = (unsigned)((8 + (lane >> 3)) * 4096 + (((lane & 7) ^ ((4 + (lane >> 4)) & 7)) << 4));
        const unsigned vL = (unsigned)(lane * 16);
        auto dKV = [&](int i, int st) {
            const unsigned dk = lds0 + st * M_KS + g * 1024;
            glds16(vL, kvb + (size_t)(i * 4096 + 4096 + g * 1024), dk);
            glds16(vL, kvb + (size_t)((4 << 20) + i * 4096 + 1024 + g * 1024), dk + 4096);
        };
        auto dG = [&](int it, int st) {
            const int kp = __builtin_amdgcn_readlane(kptab, it);
            const unsigned d = lds0 + M_G0 + st * M_GS;
            const char* c = cb + kp * 128; const char* a = ab + (size_t)kp * 2048;
            glds16(vC0, c, d + g * 4096); glds16(vC1, c, d + g * 4096 + 1024); glds16(vC0, c + 65536, d + g * 4096 + 2048); glds16(vC1, c + 65536, d + g * 4096 + 3072);
#pragma unroll
            for (int i = 0; i < 4; ++i) glds16(vL, a + (i >> 1) * 65536 + (i & 1) * 1024, d + M_AB + g * 4096 + i * 1024);
        };
        dKV(-1, 3); dKV(0, 0); dG(0, 0);
        wait_bar<10>();
        dKV(1, 1);
        wait_bar<10>();
        for (int i = 0; i < 30; ++i) {
            dKV(i + 2, (i + 2) & 3); dG(i + 1, (i + 1) & 3);
            wait_bar<10>();
        }
        dG(31, 3);
        { const char* wsrc = (const char*)wo + (size_t)(idx * 32 + g * 8) * 2048 + (xcd & 1) * 1024;
          glds16(vL, ws + WS_G + (size_t)tt * 1024, lds0 + 4096);
#pragma unroll
          for (int i = 0; i < 8; ++i) glds16(vL, wsrc + i * 2048, lds0 + g * 1024); }
        wait_bar<17>();
        wait_bar<8>();
        asm volatile("s_waitcnt vmcnt(0)" ::: "memory");
    } else if (w >= 8) {
        const int g = w - 8, gm = g >> 1, gn = g & 1, c = lane & 15, g4 = lane >> 4;
        f32x4 y[4][4];
#pragma unroll
        for (int a = 0; a < 4; ++a)
#pragma unroll
            for (int b = 0; b < 4; ++b) y[a][b] = (f32x4){};
        bf16x8 cA[4], cB[4], fa[4];
#pragma unroll
        for (int i = 0; i < 4; ++i) { cB[i] = (bf16x8){}; fa[i] = (bf16x8){}; }
        lds_cptr ca[2][2], abp[2];
#pragma unroll
        for (int h = 0; h < 2; ++h) {
#pragma unroll
            for (int hf = 0; hf < 2; ++hf) { ca[h][hf] = (lds_cptr)lds + M_G0 + h * 2 * M_GS + (gm * 64 + c) * 128 + (((4 * hf + g4) ^ ((c >> 1) & 7)) << 4); PIN(ca[h][hf]); }
            abp[h] = lp + M_G0 + h * 2 * M_GS + M_AB + gn * 8192; PIN(abp[h]);
        }
#define G_STEP(CP, CC, ST, HF) do { \
            _Pragma("unroll") for (int s_ = 0; s_ < 4; ++s_) { \
                _Pragma("unroll") for (int m = 0; m < 4; ++m) { \
                    if (s_ < 2) y[s_ + 2][m] = MFMA16(fa[s_ + 2], CP[m], y[s_ + 2][m]); else y[s_ - 2][m] = MFMA16(fa[s_ - 2], CC[m], y[s_ - 2][m]); \
                    if (s_ == 0) CC[m] = ld128(ca[(ST) >> 1][HF] + ((ST) & 1) * M_GS + m * 2048); \
                    if (m == 0) fa[s_] = ld128(abp[(ST) >> 1] + ((ST) & 1) * M_GS + s_ * 2048 + (HF) * 1024); \
                    SBAR(); } } } while (0)
#define G_ITER(st) do { bar(); G_STEP(cB, cA, st, 0); G_STEP(cA, cB, st, 1); } while (0)
        bar(); bar();
        LGKM0();
        for (int it = 0; it < 8; ++it) { G_ITER(0); G_ITER(1); G_ITER(2); G_ITER(3); }
#pragma unroll
        for (int a = 2; a < 4; ++a)
#pragma unroll
            for (int m = 0; m < 4; ++m) y[a][m] = MFMA16(fa[a], cB[m], y[a][m]);
#undef G_ITER
#undef G_STEP
#pragma unroll
        for (int m = 0; m < 4; ++m) {
            float gy = *(const LAS float*)(lds + 4096 + (gm * 64 + m * 16 + c) * 8);
            if (part) gy = -gy;
#pragma unroll
            for (int e = 0; e < 2; ++e) {
                char* yp = ws + WS_Y + (((size_t)part * 64 + tt * 4 + gm * 2 + (m >> 1)) * 32 + et * 4 + gn * 2 + e) * 2048
                           + (g4 >> 1) * 1024 + ((g4 & 1) * 32 + (m & 1) * 16 + c) * 16;
                ST16(yp, pack44(y[2 * e][m], y[2 * e + 1][m], gy));
            }
        }
    } else {
        const int qt = (idx & 7) * 8 + w;
        bf16x8 qf[2];
        { const char* qp = ws + WS_Q + (size_t)(head * 64 + qt) * 2048 + lane * 16;
          qf[0] = *(const bf16x8*)qp; qf[1] = *(const bf16x8*)(qp + 1024); }
        const float omg = *(const float*)(ws + WS_G + (size_t)(qt * 32 + r) * 8 + 4);
        bf16x8 sel; { const short one = (((lane & 1) == ((lane >> 4) & 1))) ? (short)0x3f80 : (short)0;
#pragma unroll
            for (int i = 0; i < 8; ++i) sel[i] = one; }
        f32x16 o = (f32x16){}; f32x4 l4 = (f32x4){};
        f32x16 negm, sA, sB;
        bf16x8 kf[2], vf0, vf1;
        u32x4 p0, p1;
        bar();
        DIAG_MARK(0);
        kf[0] = ld128(lp + 3 * M_KS); kf[1] = ld128(lp + 3 * M_KS + 1024);
        sA = MFMA32(kf[0], qf[0], (f32x16){}); sA = MFMA32(kf[1], qf[1], sA);
        { float m = sA[0];
#pragma unroll
          for (int i = 1; i < 16; ++i) m = fmaxf(m, sA[i]);
          m = fmaxf(m, __shfl_xor(m, 32));
#pragma unroll
          for (int i = 0; i < 16; ++i) { negm[i] = -m; sA[i] -= m; } }
        kf[0] = ld128(lp + 3 * M_KS + 2048); kf[1] = ld128(lp + 3 * M_KS + 3072);
        vf0 = ld128(lp + 3 * M_KS + 4096 + 3072);
        vf1 = vf0; p1 = (u32x4){0u, 0u, 0u, 0u};
#define EN(S, i, n) do { _Pragma("unroll") for (int e_ = 0; e_ < n; ++e_) S[i + e_] = EX2(S[i + e_]); } while (0)
#define A_STEP(SC, SN, v1o, kn, v0n, DO_QK) do { \
            if (DO_QK) { SN = MFMA32(kf[0], qf[0], negm); PIN(SN); } SBAR(); \
            EN(SC, 0, 3); PIN(SC); SBAR(); \
            o = MFMA32(vf1, PB(p1), o); PIN(o); SBAR(); \
            EN(SC, 3, 3); PIN(SC); SBAR(); \
            if (DO_QK) { SN = MFMA32(kf[1], qf[1], SN); PIN(SN); } SBAR(); \
            vf1 = ld128(lp + (v1o)); kf[0] = ld128(lp + (kn)); kf[1] = ld128(lp + (kn) + 1024); SBAR(); \
            EN(SC, 6, 2); p0[0] = cvtpk(SC[0], SC[1]); p0[1] = cvtpk(SC[2], SC[3]); PIN(SC); PIN(p0); SBAR(); \
            l4 = MFMA16(sel, PB(p1), l4); PIN(l4); SBAR(); \
            p0[2] = cvtpk(SC[4], SC[5]); p0[3] = cvtpk(SC[6], SC[7]); PIN(p0); SBAR(); EN(SC, 8, 2); PIN(SC); SBAR(); \
            o = MFMA32(vf0, PB(p0), o); PIN(o); SBAR(); \
            vf0 = ld128(lp + (v0n)); SBAR(); \
            EN(SC, 10, 3); PIN(SC); SBAR(); \
            l4 = MFMA16(sel, PB(p0), l4); PIN(l4); SBAR(); \
            EN(SC, 13, 3); \
            _Pragma("unroll") for (int i = 0; i < 4; ++i) p1[i] = cvtpk(SC[8 + 2 * i], SC[9 + 2 * i]); \
            PIN(p1); SBAR(); \
        } while (0)
#define A_ITER(st) do { \
            constexpr int b0 = (st) * M_KS; \
            bar(); \
            A_STEP(sA, sB, b0 + 4096, b0, b0 + 4096 + 1024, true); \
            A_STEP(sB, sA, b0 + 4096 + 2048, b0 + 2048, b0 + 4096 + 3072, true); \
            } while (0)
        LGKM0();
        for (int it = 0; it < 8; ++it) { A_ITER(0); A_ITER(1); A_ITER(2); A_ITER(3); }
        bar();
#undef A_ITER
#undef A_STEP
        o = MFMA32(vf1, PB(p1), o); l4 = MFMA16(sel, PB(p1), l4);
        DIAG_MARK(1);
        float l = ((lane >> 4) & 1) ? l4[1] : l4[0];
        bool ok = l < 1.0e30f;
#pragma unroll
        for (int i = 0; i < 16; ++i) ok = ok && (fabsf(o[i]) < 3.0e38f);
        if (!__all(ok)) {
            float m = -INFINITY, ls = 0.f; o = (f32x16){};
            const char* gp = kvb + lane * 16;
            for (int t = 0; t < 64; ++t) {
                const bf16x8 k0 = *(const bf16x8*)(gp + t * 2048), k1 = *(const bf16x8*)(gp + t * 2048 + 1024);
                const bf16x8 v0 = *(const bf16x8*)(gp + (4 << 20) + t * 2048), v1 = *(const bf16x8*)(gp + (4 << 20) + t * 2048 + 1024);
                f32x16 s = MFMA32(k0, qf[0], (f32x16){}); s = MFMA32(k1, qf[1], s);
                float tm = s[0];
#pragma unroll
                for (int i = 1; i < 16; ++i) tm = fmaxf(tm, s[i]);
                tm = fmaxf(tm, __shfl_xor(tm, 32));
                const float mn = fmaxf(m, tm), al = EX2(m - mn);
                m = mn; ls *= al;
#pragma unroll
                for (int i = 0; i < 16; ++i) { o[i] *= al; s[i] = EX2(s[i] - mn); ls += s[i]; }
                u32x4 a0, a1;
#pragma unroll
                for (int i = 0; i < 4; ++i) { a0[i] = cvtpk(s[2 * i], s[2 * i + 1]); a1[i] = cvtpk(s[8 + 2 * i], s[9 + 2 * i]); }
                o = MFMA32(v0, PB(a0), o); o = MFMA32(v1, PB(a1), o);
            }
            l = ls + __shfl_xor(ls, 32);
        }
        const float sc = omg / l;
        char* op = ws + WS_O + ((size_t)qt * 64 + 2 * head) * 1024 + lane * 16;
#pragma unroll
        for (int s = 0; s < 2; ++s) ST16(op + s * 1024, pack8(o, s, sc));
    }
    DIAG_END(1, ws);
}

constexpr int F_ST = 24576, F_NST = 5, F_LDS = F_ST * F_NST;
__global__ __launch_bounds__(768, 3) void k_out(const char* __restrict__ ws, const u16* __restrict__ wo, u16* __restrict__ out) {
    extern __shared__ __attribute__((aligned(16))) char smem[];
    LAS char* lds = (LAS char*)smem;
    DIAG_BEGIN();
    const int tid = threadIdx.x, lane = tid & 63, w = __builtin_amdgcn_readfirstlane(tid >> 6);
    const int xcd = blockIdx.x & 7, idx = blockIdx.x >> 3;
    const int tt = (xcd >> 1) * 4 + (idx >> 3), et = (xcd & 1) * 8 + (idx & 7);
    if (w >= 8) {
        const int g = w - 8;
        const int la = ((idx >> 3) - (idx & 7)) & 3, lc = (idx >> 1) & 3;
        const int kptab = lf_order(lane & 15, la, lc, 4);
        const unsigned lds0 = (unsigned)(uintptr_t)lds;
        const unsigned vL = (unsigned)(lane * 16);
        const unsigned vT = (unsigned)((lane >> 3) * 2048 + (((lane & 7) ^ (((lane >> 4) & 1) << 2)) << 4));
        const char* abase = ws + WS_O + (size_t)(tt * 4 + g) * 65536;
        const char* bbase = (const char*)wo + (size_t)g * 16 * 2048 + et * 128;
        auto dma = [&](int kt, int st) {
            const int kp = __builtin_amdgcn_readlane(kptab, kt);
            const unsigned d = lds0 + st * F_ST; const char* a = abase + (size_t)kp * 4096; const char* b = bbase + (size_t)kp * 64 * 2048;
            glds16(vT, b, d + 16384 + g * 2048); glds16(vT, b + 16384, d + 16384 + g * 2048 + 1024);
#pragma unroll
            for (int i = 0; i < 4; ++i) glds16(vL, a + i * 1024, d + g * 4096 + i * 1024);
        };
        dma(0, 0); dma(1, 1);
        wait_bar<6>(); dma(2, 2); dma(3, 3);
        int sl = 4;
        for (int kt = 1; kt < 13; ++kt) { wait_bar<12>(); dma(kt + 3, sl); sl = NX5(sl); }
        wait_bar<12>(); wait_bar<6>(); wait_bar<0>();
    } else {
        const int wm = w >> 1, wn = w & 1, r = lane & 31, hh = lane >> 5;
        const int aoff = wm * 4096 + lane * 16;
        const int q4 = (lane & 15) >> 2, p4 = lane & 3, cg = (lane >> 4) & 1, pi4 = ((p4 & 1) << 1) | (p4 >> 1);
        const int tb = 16384 + (8 * hh + q4) * 128 + ((wn ^ (q4 >> 1)) << 6) + cg * 32 + pi4 * 8;
        f32x16 acc = (f32x16){};
        bf16x8 a0[2], b0[2], a1[2], b1[2];
        auto rd = [&](bf16x8 (&fa)[2], bf16x8 (&fb)[2], int st, int s2, int i) {
            lds_cptr b = (lds_cptr)lds + st * F_ST;
            fa[i] = ld128(b + aoff + (2 * s2 + i) * 1024); fb[i] = ldtr(b + tb + (2 * s2 + i) * 2048);
        };
        a1[0] = a1[1] = b1[0] = b1[1] = (bf16x8){};
#define F_ITER(st) do { \
            bar(); \
            acc = MFMA32(b1[0], a1[0], acc); rd(a0, b0, st, 0, 0); SBAR(); \
            acc = MFMA32(b1[1], a1[1], acc); rd(a0, b0, st, 0, 1); SBAR(); \
            acc = MFMA32(b0[0], a0[0], acc); rd(a1, b1, st, 1, 0); SBAR(); \
            acc = MFMA32(b0[1], a0[1], acc); rd(a1, b1, st, 1, 1); SBAR(); \
            } while (0)
        LGKM0();
        for (int kt = 0; kt < 2; ++kt) { F_ITER(0); F_ITER(1); F_ITER(2); F_ITER(3); F_ITER(4); }
        F_ITER(0); F_ITER(1);
        const char* yp = ws + WS_Y + (((size_t)tt * 4 + wm) * 32 + et * 2 + wn) * 2048 + lane * 16;
        const u32x4 ya0 = *(const u32x4*)yp, ya1 = *(const u32x4*)(yp + 1024);
        const u32x4 yb0 = *(const u32x4*)(yp + (4u << 20)), yb1 = *(const u32x4*)(yp + (4u << 20) + 1024);
        F_ITER(2); F_ITER(3); F_ITER(4); F_ITER(0);
        acc = MFMA32(b1[0], a1[0], acc); acc = MFMA32(b1[1], a1[1], acc);
#undef F_ITER
        DIAG_MARK(1);
#pragma unroll
        for (int i = 0; i < 4; ++i) {
            acc[2 * i] += __uint_as_float(ya0[i] << 16) + __uint_as_float(yb0[i] << 16);
            acc[2 * i + 1] += __uint_as_float(ya0[i] & 0xffff0000u) + __uint_as_float(yb0[i] & 0xffff0000u);
            acc[8 + 2 * i] += __uint_as_float(ya1[i] << 16) + __uint_as_float(yb1[i] << 16);
            acc[9 + 2 * i] += __uint_as_float(ya1[i] & 0xffff0000u) + __uint_as_float(yb1[i] & 0xffff0000u);
        }
        const int row = tt * 128 + wm * 32 + r;
        u16* op = out + (size_t)row * 1024 + et * 64 + wn * 32 + hh * 8;
#pragma unroll
        for (int s = 0; s < 2; ++s) *(u32x4*)(op + s * 16) = pack8(acc, s, 1.f);
    }
    DIAG_END(2, ws);
}

extern "C" void kernel_launch(void* const* d_in, const int* in_sizes, int n_in, void* d_out, int out_size, void* d_ws, size_t ws_size,
                              hipStream_t stream) {
    const u16* x = (const u16*)d_in[0]; const u16* cr = (const u16*)d_in[1]; const u16* ci = (const u16*)d_in[2];
    const u16* hr = (const u16*)d_in[3]; const u16* hi = (const u16*)d_in[4];
    const u16* wqkv = (const u16*)d_in[6]; const u16* wo = (const u16*)d_in[7]; const u16* gw = (const u16*)d_in[8];
    char* ws = (char*)d_ws;
    (void)hipFuncSetAttribute((const void*)k_proj, hipFuncAttributeMaxDynamicSharedMemorySize, P_LDS);
    (void)hipFuncSetAttribute((const void*)k_mid, hipFuncAttributeMaxDynamicSharedMemorySize, M_LDS);
    (void)hipFuncSetAttribute((const void*)k_out, hipFuncAttributeMaxDynamicSharedMemorySize, F_LDS);
    k_proj<<<256, 512, P_LDS, stream>>>(x, wqkv, hr, hi, gw, ws);
    k_mid<<<256, 1024, M_LDS, stream>>>(cr, ci, wo, ws);
    k_out<<<256, 768, F_LDS, stream>>>(ws, wo, (u16*)d_out);
}
```

```cpp
#include <hip/hip_runtime.h>
#include <hip/hip_bf16.h>

#define P_NOCOMP 0
#define P_NODMA 0
#define P_NOGATE 0
typedef unsigned short u16;
typedef short bf16x8 __attribute__((ext_vector_type(8)));
typedef short s16x4 __attribute__((ext_vector_type(4)));
typedef float f32x16 __attribute__((ext_vector_type(16)));
typedef float f32x4 __attribute__((ext_vector_type(4)));
typedef float f32x2 __attribute__((ext_vector_type(2)));
typedef unsigned u32x4 __attribute__((ext_vector_type(4)));
#define LAS __attribute__((address_space(3)))
typedef LAS const char* lds_cptr;

constexpr size_t WS_Q = 0;
constexpr size_t WS_KV = 4u << 20;
constexpr size_t WS_AB = 12u << 20;
constexpr size_t WS_O = 20u << 20;
constexpr size_t WS_Y = 24u << 20;
constexpr size_t WS_G = 40u << 20;

#define DIAG_BEGIN()
#define DIAG_MARK(i)
#define DIAG_END(kid, wsp)

#define MFMA32(a, b, c) __builtin_amdgcn_mfma_f32_32x32x16_bf16((a), (b), (c), 0, 0, 0)
#define MFMA16(a, b, c) __builtin_amdgcn_mfma_f32_16x16x32_bf16((a), (b), (c), 0, 0, 0)
#define SBAR() __builtin_amdgcn_sched_barrier(0)
#define PIN(x) asm volatile("" : "+v"(x))
#define EX2(v) __builtin_amdgcn_exp2f(v)
#define PB(p) __builtin_bit_cast(bf16x8, p)
#define LGKM0() __builtin_amdgcn_s_waitcnt(0xC07F)

__device__ __forceinline__ unsigned cvtpk(float lo, float hi) {
    typedef __bf16 bf16x2_t __attribute__((ext_vector_type(2)));
    return __builtin_bit_cast(unsigned, __builtin_convertvector((f32x2){lo, hi}, bf16x2_t)); }
__device__ __forceinline__ float bfr(float f) { return __uint_as_float(cvtpk(f, 0.f) << 16); }

__device__ __forceinline__ void glds16(unsigned voff, const void* sbase, unsigned ldsdst) {
    asm volatile("s_mov_b32 m0, %2\n\ts_nop 0\n\tglobal_load_lds_dwordx4 %0, %1" :: "v"(voff), "s"(sbase), "s"(ldsdst) : "memory");
}
template <int N> __device__ __forceinline__ void wait_bar() {
    SBAR();
    asm volatile("s_waitcnt vmcnt(%0)\n\ts_barrier" :: "n"(N) : "memory");
    SBAR();
}
__device__ __forceinline__ void bar() { SBAR(); asm volatile("s_barrier" ::: "memory"); SBAR(); }
__device__ __forceinline__ void bar_l1() {
    SBAR();
    asm volatile("s_waitcnt lgkmcnt(1)\n\ts_barrier" ::: "memory");
    SBAR();
}
__device__ __forceinline__ bf16x8 ld128(lds_cptr p) { return *(const LAS bf16x8*)p; }
__device__ __forceinline__ bf16x8 ldtr(lds_cptr p) {
    s16x4 lo = __builtin_amdgcn_ds_read_tr16_b64_v4i16((LAS s16x4*)p);
    s16x4 hi = __builtin_amdgcn_ds_read_tr16_b64_v4i16((LAS s16x4*)(p + 512));
    return (bf16x8){lo[0], lo[1], lo[2], lo[3], hi[0], hi[1], hi[2], hi[3]};
}
__device__ __forceinline__ u32x4 pack8(const f32x16& a, int s, float sc) {
    u32x4 w;
    w[0] = cvtpk(a[8 * s + 0] * sc, a[8 * s + 1] * sc); w[1] = cvtpk(a[8 * s + 2] * sc, a[8 * s + 3] * sc);
    w[2] = cvtpk(a[8 * s + 4] * sc, a[8 * s + 5] * sc); w[3] = cvtpk(a[8 * s + 6] * sc, a[8 * s + 7] * sc);
    return w;
}
#define ST16(p, v) do { const u32x4 v_ = (v); asm volatile("global_store_dwordx4 %0, %1, off sc0 sc1\n\ts_nop 1" :: "v"(p), "v"(v_) : "memory"); } while (0)
__device__ __forceinline__ int swap23(int i) { return (i & ~12) | ((i & 4) << 1) | ((i & 8) >> 1); }

__device__ __forceinline__ int lf_order(int tau, int a, int c, int G) {
    if (tau == 0) return a;
    const int g = (tau - 1) >> 2, pos = (tau - 1) & 3;
    if (g != G - 1 && pos == c) return 4 * (g + 1) + a;
    const int b = (g == G - 1 || pos < c) ? pos : pos - 1;
    return 4 * g + (b < a ? b : b + 1);
}
#define NX3(v) ((v) == 2 ? 0 : (v) + 1)
#define NX5(v) ((v) == 4 ? 0 : (v) + 1)

constexpr int P_RS = 32768, P_TS = 12288, P_TB = 3 * P_RS, P_GW = P_TB + 5 * P_TS, P_LDS = P_GW + 2048;
__device__ __forceinline__ int rswz(int row) { return ((row >> 1) & 1) | (((row >> 3) & 3) << 1); }
__device__ __forceinline__ u32x4 pack44(const f32x4& a, const f32x4& b, float sc) {
    return (u32x4){cvtpk(a[0] * sc, a[1] * sc), cvtpk(a[2] * sc, a[3] * sc), cvtpk(b[0] * sc, b[1] * sc), cvtpk(b[2] * sc, b[3] * sc)};
}
__global__ __launch_bounds__(512, 2) void k_proj(const u16* __restrict__ x, const u16* __restrict__ wqkv, const u16* __restrict__ hr,
                                                  const u16* __restrict__ hi, const u16* __restrict__ gw, char* __restrict__ ws) {
    extern __shared__ __attribute__((aligned(16))) char smem[];
    LAS char* lds = (LAS char*)smem;
    DIAG_BEGIN();
    const int tid = threadIdx.x, lane = tid & 63, w = __builtin_amdgcn_readfirstlane(tid >> 6);
    const int xcd = blockIdx.x & 7, idx = blockIdx.x >> 3;
    const int rt = (xcd & 1) * 8 + (idx & 7), j = (xcd >> 1) * 4 + (idx >> 3);
    if (w >= 4) {
        const int g = w - 4;
        const int la = ((idx >> 3) - (idx & 7)) & 3, lc = (idx >> 1) & 3;
        const int kptab = lf_order(lane & 15, la, lc, 4);
        const unsigned lds0 = (unsigned)(uintptr_t)lds;
        const char* xb = (const char*)x + (size_t)(rt * 128 + g * 32) * 2048;
        const char* hb = (const char*)(j < 8 ? hr : hi) + (size_t)((j & 7) * 128 + g * 32) * 2048;
        const char* wb = (const char*)wqkv + (size_t)g * 8 * 6144 + j * 128;
        unsigned vR[4];
#pragma unroll
        for (int i = 0; i < 4; ++i) vR[i] = (unsigned)((8 * i + (lane >> 3)) * 2048 + (((lane & 7) ^ rswz(8 * i + (lane >> 3))) << 4));
        const unsigned vT = (unsigned)((lane >> 3) * 6144 + (((lane & 7) ^ (((lane >> 4) & 1) << 2) ^ ((g & 1) << 1)) << 4));
        auto dT = [&](int kp, int hf, int ts) {
            const char* b = wb + (size_t)(kp * 64 + hf * 32) * 6144; const unsigned d = lds0 + P_TB + ts * P_TS + g * 1024;
            if (P_NODMA) return;
            glds16(vT, b, d); glds16(vT, b + 2048, d + 4096); glds16(vT, b + 4096, d + 8192);
        };
        auto dR = [&](int kp, int hf, int rs) {
            const char* b = (hf ? hb : xb) + kp * 128; const unsigned d = lds0 + rs * P_RS + hf * 16384 + g * 4096;
#pragma unroll
            for (int i = 0; i < 4; ++i) if (!P_NODMA) glds16(vR[i], b, d + i * 1024);
        };
        const int grow = 8 * j + 2 * g + ((lane >> 3) & 1);
        lds_cptr gxa = (lds_cptr)lds + grow * 128 + (((lane & 7) ^ rswz(grow)) << 4);
        lds_cptr gwa = (lds_cptr)lds + P_GW + (lane & 7) * 16;
        float z = 0.f;
        auto gate = [&](int kp, int rs) {
            if (P_NOGATE) return;
            const u32x4 a = *(const LAS u32x4*)(gxa + rs * P_RS), b = *(const LAS u32x4*)(gwa + kp * 128);
#pragma unroll
            for (int e = 0; e < 4; ++e) {
                z = fmaf(__uint_as_float(a[e] << 16), __uint_as_float(b[e] << 16), z);
                z = fmaf(__uint_as_float(a[e] & 0xffff0000u), __uint_as_float(b[e] & 0xffff0000u), z);
            }
        };
        { const int k0 = __builtin_amdgcn_readlane(kptab, 0), k1 = __builtin_amdgcn_readlane(kptab, 1), k2 = __builtin_amdgcn_readlane(kptab, 2);
          dR(k0, 0, 0); dT(k0, 0, 0); dR(k0, 1, 0);
          glds16((unsigned)(lane * 16), (const char*)gw + (g & 1) * 1024, lds0 + P_GW + (g & 1) * 1024);
          dT(k0, 1, 1);
          wait_bar<3>();
          dT(k1, 0, 2); dR(k1, 0, 1);
          wait_bar<7>();
          dT(k1, 1, 3); dR(k1, 1, 1); dT(k2, 0, 4); dR(k2, 0, 2);
          gate(k0, 0); }
        int rsp = 2, tsp = 0;
        for (int kt = 1; kt < 14; ++kt) {
            const int kp0 = __builtin_amdgcn_readlane(kptab, kt), kp1 = __builtin_amdgcn_readlane(kptab, kt + 1), kp2 = __builtin_amdgcn_readlane(kptab, kt + 2);
            wait_bar<7>();
            dT(kp1, 1, tsp); dR(kp1, 1, rsp); tsp = NX5(tsp); rsp = NX3(rsp);
            gate(kp0, NX3(rsp));
            wait_bar<18>();
            dT(kp2, 0, tsp); dR(kp2, 0, rsp); tsp = NX5(tsp);
        }
        { const int kp0 = __builtin_amdgcn_readlane(kptab, 14), kp1 = __builtin_amdgcn_readlane(kptab, 15);
          wait_bar<7>();
          dT(kp1, 1, tsp); dR(kp1, 1, rsp);
          gate(kp0, 2);
          wait_bar<18>();
          wait_bar<0>();
          gate(kp1, 0);
          bar(); }
        z += __shfl_xor(z, 1); z += __shfl_xor(z, 2); z += __shfl_xor(z, 4);
        const float gt = bfr(1.f / (1.f + __expf(-bfr(z))));
        if ((lane & 55) == 0) *(f32x2*)(ws + WS_G + (size_t)(rt * 128 + grow) * 8) = (f32x2){gt, bfr(1.f - gt)};
    } else {
        const int wm = w >> 1, wn = w & 1, c = lane & 15, g4 = lane >> 4;
        const int sg0 = 8 * (c >> 2) + (c & 3);
        int ra[2], tb[2];
#pragma unroll
        for (int hf = 0; hf < 2; ++hf) ra[hf] = sg0 * 128 + (((4 * hf + g4) ^ rswz(sg0)) << 4);
        { const int q4 = c >> 2, p4 = c & 3;
#pragma unroll
          for (int dt = 0; dt < 2; ++dt)
              tb[dt] = P_TB + (8 * g4 + q4) * 128 + ((((wn * 4 + dt * 2) ^ ((q4 >> 1) << 2) ^ ((g4 & 1) << 1)) + (p4 >> 1)) << 4) + (p4 & 1) * 8; }
        const int xoff = wm * 8192, hoff = 16384 + wn * 8192;
        f32x4 acc[10][4];
#pragma unroll
        for (int b = 0; b < 10; ++b)
#pragma unroll
            for (int m = 0; m < 4; ++m) acc[b][m] = (f32x4){};
        bf16x8 xA[4], xB[4], fb[5];
#pragma unroll
        for (int m = 0; m < 4; ++m) xB[m] = (bf16x8){};
#pragma unroll
        for (int b = 0; b < 5; ++b) fb[b] = (bf16x8){};
        auto rdB = [&](int b, int half, lds_cptr rb, lds_cptr t0, lds_cptr t1) {
            if (b < 6) {
                const s16x4 v = __builtin_amdgcn_ds_read_tr16_b64_v4i16((LAS s16x4*)(((b & 1) ? t1 : t0) + (b >> 1) * 4096 + half * 512));
#pragma unroll
                for (int e = 0; e < 4; ++e) fb[b % 5][4 * half + e] = v[e];
            } else if (half == 0) fb[b % 5] = ld128(rb + hoff + ((b - 6) >> 1) * 4096 + (b & 1) * 512);
        };
        auto mm = [&](int b, bf16x8 (&xf)[4], int m) {
            if (b < 4) acc[b][m] = MFMA16(fb[b % 5], xf[m], acc[b][m]);
            else acc[b][m] = MFMA16(xf[m], fb[b % 5], acc[b][m]);
        };
#define P_HALF(XP, XC, RS, TS, HF) do { \
            lds_cptr rb_ = (lds_cptr)lds + (RS) * P_RS + ra[HF]; \
            lds_cptr t0_ = (lds_cptr)lds + (TS) * P_TS + tb[0]; lds_cptr t1_ = (lds_cptr)lds + (TS) * P_TS + tb[1]; \
            bar(); \
            _Pragma("unroll") for (int s_ = 0; s_ < 10; ++s_) { \
                _Pragma("unroll") for (int m = 0; m < 4; ++m) { \
                    if (s_ < 3) mm(s_ + 7, XP, m); else mm(s_ - 3, XC, m); \
                    SBAR(); \
                    if (s_ < 2 && (m == 0 || m == 3)) { const int xi_ = 2 * s_ + (m ? 1 : 0); XC[xi_] = ld128(rb_ + xoff + (xi_ >> 1) * 4096 + (xi_ & 1) * 512); } \
                    if (m == 1 || m == 2) rdB(s_, m - 1, rb_, t0_, t1_); \
                    SBAR(); } } } while (0)
        int rs = 0, ts = 0;
        LGKM0();
        for (int kt = 0; kt < 16; ++kt) {
            const int ts1 = NX5(ts);
            P_HALF(xB, xA, rs, ts, 0);
            P_HALF(xA, xB, rs, ts1, 1);
            ts = NX5(ts1); rs = NX3(rs);
        }
#pragma unroll
        for (int b = 7; b < 10; ++b)
#pragma unroll
            for (int m = 0; m < 4; ++m) mm(b, xB, m);
#undef P_HALF
        DIAG_MARK(1);
        const int head = 2 * j + wn;
        const float qs = 0.17677669529663687f * 1.4426950408889634f;
        const int fo = (g4 >> 1) * 1024 + (g4 & 1) * 512;
#pragma unroll
        for (int gr = 0; gr < 2; ++gr) {
            const int t32 = rt * 4 + wm * 2 + gr;
            char* qp = ws + WS_Q + (size_t)(head * 64 + t32) * 2048 + fo;
            char* kp = ws + WS_KV + (size_t)(head * 64 + t32) * 2048 + fo;
#pragma unroll
            for (int a = 0; a < 2; ++a) {
                ST16(qp + (sg0 + 4 * a) * 16, pack44(acc[0][2 * gr + a], acc[1][2 * gr + a], qs));
                ST16(kp + (16 * (c >> 3) + 8 * a + (c & 7)) * 16, pack44(acc[2][2 * gr + a], acc[3][2 * gr + a], 1.f));
            }
#pragma unroll
            for (int dt = 0; dt < 2; ++dt)
                ST16(kp + (4u << 20) + (16 * dt + swap23(c)) * 16, pack44(acc[4 + dt][2 * gr], acc[4 + dt][2 * gr + 1], 1.f));
            char* abp = ws + WS_AB + ((size_t)(j * 8 + wn * 4) * 64 + t32) * 1024 + lane * 16;
#pragma unroll
            for (int b = 0; b < 4; ++b) ST16(abp + (size_t)b * 65536, pack44(acc[6 + b][2 * gr], acc[6 + b][2 * gr + 1], 1.f));
        }
    }
    DIAG_END(0, ws);
}

constexpr int M_NST = 4, M_KS = 8192, M_G0 = M_NST * M_KS, M_GS = 32768, M_AB = 16384, M_LDS = M_G0 + M_NST * M_GS;
__global__ __launch_bounds__(1024, 4) void k_mid(const u16* __restrict__ cr, const u16* __restrict__ ci, const u16* __restrict__ wo, char* __restrict__ ws) {
    extern __shared__ __attribute__((aligned(16))) char smem[];
    LAS char* lds = (LAS char*)smem;
    DIAG_BEGIN();
    const int tid = threadIdx.x, lane = tid & 63, w = __builtin_amdgcn_readfirstlane(tid >> 6);
    const int xcd = blockIdx.x & 7, idx = blockIdx.x >> 3;
    const int head = xcd * 4 + (idx >> 3);
    const int part = xcd >> 2, tt = ((xcd >> 1) & 1) * 8 + (idx & 7), et = (xcd & 1) * 4 + (idx >> 3);
    const int r = lane & 31, hh = lane >> 5;
    const char* kvb = ws + WS_KV + (size_t)head * 64 * 2048;
    lds_cptr lp = (lds_cptr)lds + lane * 16;
    if (w >= 12) {
        const int g = w - 12;
        const int la = ((idx >> 3) - (idx & 7)) & 3, lc = (idx >> 1) & 3;
        const int kptab = lf_order(lane & 31, la, lc, 8);
        const unsigned lds0 = (unsigned)(uintptr_t)lds;
        const char* cb = (const char*)(part ? ci : cr) + (size_t)(tt * 128 + 32 * g) * 4096;
        const char* ab = ws + WS_AB + (size_t)(part * 64 + et * 8 + 2 * g) * 65536;
        const unsigned vC0 = (unsigned)((lane >> 3) * 4096 + (((lane & 7) ^ ((lane >> 4) & 7)) << 4));
        const unsigned vC1 = (unsigned)((8 + (lane >> 3)) * 4096 + (((lane & 7) ^ ((4 + (lane >> 4)) & 7)) << 4));
        const unsigned vL = (unsigned)(lane * 16);
        auto dKV = [&](int i, int st) {
            const unsigned dk = lds0 + st * M_KS + g * 1024;
            glds16(vL, kvb + (size_t)(i * 4096 + 4096 + g * 1024), dk);
            glds16(vL, kvb + (size_t)((4 << 20) + i * 4096 + 1024 + g * 1024), dk + 4096);
        };
        auto dG = [&](int it, int st) {
            const int kp = __builtin_amdgcn_readlane(kptab, it);
            const unsigned d = lds0 + M_G0 + st * M_GS;
            const char* c = cb + kp * 128; const char* a = ab + (size_t)kp * 2048;
            glds16(vC0, c, d + g * 4096); glds16(vC1, c, d + g * 4096 + 1024); glds16(vC0, c + 65536, d + g * 4096 + 2048); glds16(vC1, c + 65536, d + g * 4096 + 3072);
#pragma unroll
            for (int i = 0; i < 4; ++i) glds16(vL, a + (i >> 1) * 65536 + (i & 1) * 1024, d + M_AB + g * 4096 + i * 1024);
        };
        dKV(-1, 3); dKV(0, 0); dG(0, 0);
        wait_bar<10>();
        dKV(1, 1);
        wait_bar<10>();
        for (int i = 0; i < 30; ++i) {
            dKV(i + 2, (i + 2) & 3); dG(i + 1, (i + 1) & 3);
            wait_bar<10>();
        }
        dG(31, 3);
        { const char* wsrc = (const char*)wo + (size_t)(idx * 32 + g * 8) * 2048 + (xcd & 1) * 1024;
          glds16(vL, ws + WS_G + (size_t)tt * 1024, lds0 + 4096);
#pragma unroll
          for (int i = 0; i < 8; ++i) glds16(vL, wsrc + i * 2048, lds0 + g * 1024); }
        wait_bar<17>();
        wait_bar<8>();
        asm volatile("s_waitcnt vmcnt(0)" ::: "memory");
    } else if (w >= 8) {
        const int g = w - 8, gm = g >> 1, gn = g & 1, c = lane & 15, g4 = lane >> 4;
        f32x4 y[4][4];
#pragma unroll
        for (int a = 0; a < 4; ++a)
#pragma unroll
            for (int b = 0; b < 4; ++b) y[a][b] = (f32x4){};
        bf16x8 cA[4], cB[4], fa[4];
#pragma unroll
        for (int i = 0; i < 4; ++i) { cB[i] = (bf16x8){}; fa[i] = (bf16x8){}; }
        lds_cptr ca[2][2], abp[2];
#pragma unroll
        for (int h = 0; h < 2; ++h) {
#pragma unroll
            for (int hf = 0; hf < 2; ++hf) { ca[h][hf] = (lds_cptr)lds + M_G0 + h * 2 * M_GS + (gm * 64 + c) * 128 + (((4 * hf + g4) ^ ((c >> 1) & 7)) << 4); PIN(ca[h][hf]); }
            abp[h] = lp + M_G0 + h * 2 * M_GS + M_AB + gn * 8192; PIN(abp[h]);
        }
#define G_STEP(CP, CC, ST, HF) do { \
            _Pragma("unroll") for (int s_ = 0; s_ < 4; ++s_) { \
                _Pragma("unroll") for (int m = 0; m < 4; ++m) { \
                    if (s_ < 2) y[s_ + 2][m] = MFMA16(fa[s_ + 2], CP[m], y[s_ + 2][m]); else y[s_ - 2][m] = MFMA16(fa[s_ - 2], CC[m], y[s_ - 2][m]); \
                    if (s_ == 0) CC[m] = ld128(ca[(ST) >> 1][HF] + ((ST) & 1) * M_GS + m * 2048); \
                    if (m == 0) fa[s_] = ld128(abp[(ST) >> 1] + ((ST) & 1) * M_GS + s_ * 2048 + (HF) * 1024); \
                    SBAR(); } } } while (0)
#define G_ITER(st) do { bar(); G_STEP(cB, cA, st, 0); G_STEP(cA, cB, st, 1); } while (0)
        bar(); bar();
        LGKM0();
        for (int it = 0; it < 8; ++it) { G_ITER(0); G_ITER(1); G_ITER(2); G_ITER(3); }
#pragma unroll
        for (int a = 2; a < 4; ++a)
#pragma unroll
            for (int m = 0; m < 4; ++m) y[a][m] = MFMA16(fa[a], cB[m], y[a][m]);
#undef G_ITER
#undef G_STEP
#pragma unroll
        for (int m = 0; m < 4; ++m) {
            float gy = *(const LAS float*)(lds + 4096 + (gm * 64 + m * 16 + c) * 8);
            if (part) gy = -gy;
#pragma unroll
            for (int e = 0; e < 2; ++e) {
                char* yp = ws + WS_Y + (((size_t)part * 64 + tt * 4 + gm * 2 + (m >> 1)) * 32 + et * 4 + gn * 2 + e) * 2048
                           + (g4 >> 1) * 1024 + ((g4 & 1) * 32 + (m & 1) * 16 + c) * 16;
                ST16(yp, pack44(y[2 * e][m], y[2 * e + 1][m], gy));
            }
        }
    } else {
        const int qt = (idx & 7) * 8 + w;
        bf16x8 qf[2];
        { const char* qp = ws + WS_Q + (size_t)(head * 64 + qt) * 2048 + lane * 16;
          qf[0] = *(const bf16x8*)qp; qf[1] = *(const bf16x8*)(qp + 1024); }
        const float omg = *(const float*)(ws + WS_G + (size_t)(qt * 32 + r) * 8 + 4);
        bf16x8 sel; { const short one = (((lane & 1) == ((lane >> 4) & 1))) ? (short)0x3f80 : (short)0;
#pragma unroll
            for (int i = 0; i < 8; ++i) sel[i] = one; }
        f32x16 o = (f32x16){}; f32x4 l4 = (f32x4){};
        f32x16 negm, sA, sB;
        bf16x8 kf[2], vf0, vf1;
        u32x4 p0, p1;
        bar();
        DIAG_MARK(0);
        kf[0] = ld128(lp + 3 * M_KS); kf[1] = ld128(lp + 3 * M_KS + 1024);
        sA = MFMA32(kf[0], qf[0], (f32x16){}); sA = MFMA32(kf[1], qf[1], sA);
        { float m = sA[0];
#pragma unroll
          for (int i = 1; i < 16; ++i) m = fmaxf(m, sA[i]);
          m = fmaxf(m, __shfl_xor(m, 32));
#pragma unroll
          for (int i = 0; i < 16; ++i) { negm[i] = -m; sA[i] -= m; } }
        kf[0] = ld128(lp + 3 * M_KS + 2048); kf[1] = ld128(lp + 3 * M_KS + 3072);
        vf0 = ld128(lp + 3 * M_KS + 4096 + 3072);
        vf1 = vf0; p1 = (u32x4){0u, 0u, 0u, 0u};
#define EN(S, i, n) do { _Pragma("unroll") for (int e_ = 0; e_ < n; ++e_) S[i + e_] = EX2(S[i + e_]); } while (0)
#define A_STEP(SC, SN, v1o, kn, v0n, DO_QK) do { \
            if (DO_QK) { SN = MFMA32(kf[0], qf[0], negm); PIN(SN); } SBAR(); \
            EN(SC, 0, 3); PIN(SC); SBAR(); \
            o = MFMA32(vf1, PB(p1), o); PIN(o); SBAR(); \
            EN(SC, 3, 3); PIN(SC); SBAR(); \
            if (DO_QK) { SN = MFMA32(kf[1], qf[1], SN); PIN(SN); } SBAR(); \
            vf1 = ld128(lp + (v1o)); kf[0] = ld128(lp + (kn)); kf[1] = ld128(lp + (kn) + 1024); SBAR(); \
            EN(SC, 6, 2); p0[0] = cvtpk(SC[0], SC[1]); p0[1] = cvtpk(SC[2], SC[3]); PIN(SC); PIN(p0); SBAR(); \
            l4 = MFMA16(sel, PB(p1), l4); PIN(l4); SBAR(); \
            p0[2] = cvtpk(SC[4], SC[5]); p0[3] = cvtpk(SC[6], SC[7]); PIN(p0); SBAR(); EN(SC, 8, 2); PIN(SC); SBAR(); \
            o = MFMA32(vf0, PB(p0), o); PIN(o); SBAR(); \
            vf0 = ld128(lp + (v0n)); SBAR(); \
            EN(SC, 10, 3); PIN(SC); SBAR(); \
            l4 = MFMA16(sel, PB(p0), l4); PIN(l4); SBAR(); \
            EN(SC, 13, 3); \
            _Pragma("unroll") for (int i = 0; i < 4; ++i) p1[i] = cvtpk(SC[8 + 2 * i], SC[9 + 2 * i]); \
            PIN(p1); SBAR(); \
        } while (0)
#define A_ITER(st) do { \
            constexpr int b0 = (st) * M_KS; \
            bar(); \
            A_STEP(sA, sB, b0 + 4096, b0, b0 + 4096 + 1024, true); \
            A_STEP(sB, sA, b0 + 4096 + 2048, b0 + 2048, b0 + 4096 + 3072, true); \
            } while (0)
        LGKM0();
        for (int it = 0; it < 8; ++it) { A_ITER(0); A_ITER(1); A_ITER(2); A_ITER(3); }
        bar();
#undef A_ITER
#undef A_STEP
        o = MFMA32(vf1, PB(p1), o); l4 = MFMA16(sel, PB(p1), l4);
        DIAG_MARK(1);
        float l = ((lane >> 4) & 1) ? l4[1] : l4[0];
        bool ok = l < 1.0e30f;
#pragma unroll
        for (int i = 0; i < 16; ++i) ok = ok && (fabsf(o[i]) < 3.0e38f);
        if (!__all(ok)) {
            float m = -INFINITY, ls = 0.f; o = (f32x16){};
            const char* gp = kvb + lane * 16;
            for (int t = 0; t < 64; ++t) {
                const bf16x8 k0 = *(const bf16x8*)(gp + t * 2048), k1 = *(const bf16x8*)(gp + t * 2048 + 1024);
                const bf16x8 v0 = *(const bf16x8*)(gp + (4 << 20) + t * 2048), v1 = *(const bf16x8*)(gp + (4 << 20) + t * 2048 + 1024);
                f32x16 s = MFMA32(k0, qf[0], (f32x16){}); s = MFMA32(k1, qf[1], s);
                float tm = s[0];
#pragma unroll
                for (int i = 1; i < 16; ++i) tm = fmaxf(tm, s[i]);
                tm = fmaxf(tm, __shfl_xor(tm, 32));
                const float mn = fmaxf(m, tm), al = EX2(m - mn);
                m = mn; ls *= al;
#pragma unroll
                for (int i = 0; i < 16; ++i) { o[i] *= al; s[i] = EX2(s[i] - mn); ls += s[i]; }
                u32x4 a0, a1;
#pragma unroll
                for (int i = 0; i < 4; ++i) { a0[i] = cvtpk(s[2 * i], s[2 * i + 1]); a1[i] = cvtpk(s[8 + 2 * i], s[9 + 2 * i]); }
                o = MFMA32(v0, PB(a0), o); o = MFMA32(v1, PB(a1), o);
            }
            l = ls + __shfl_xor(ls, 32);
        }
        const float sc = omg / l;
        char* op = ws + WS_O + ((size_t)qt * 64 + 2 * head) * 1024 + lane * 16;
#pragma unroll
        for (int s = 0; s < 2; ++s) ST16(op + s * 1024, pack8(o, s, sc));
    }
    DIAG_END(1, ws);
}

constexpr int F_ST = 24576, F_NST = 5, F_LDS = F_ST * F_NST;
__global__ __launch_bounds__(768, 3) void k_out(const char* __restrict__ ws, const u16* __restrict__ wo, u16* __restrict__ out) {
    extern __shared__ __attribute__((aligned(16))) char smem[];
    LAS char* lds = (LAS char*)smem;
    DIAG_BEGIN();
    const int tid = threadIdx.x, lane = tid & 63, w = __builtin_amdgcn_readfirstlane(tid >> 6);
    const int xcd = blockIdx.x & 7, idx = blockIdx.x >> 3;
    const int tt = (xcd >> 1) * 4 + (idx >> 3), et = (xcd & 1) * 8 + (idx & 7);
    if (w >= 8) {
        const int g = w - 8;
        const int la = ((idx >> 3) - (idx & 7)) & 3, lc = (idx >> 1) & 3;
        const int kptab = lf_order(lane & 15, la, lc, 4);
        const unsigned lds0 = (unsigned)(uintptr_t)lds;
        const unsigned vL = (unsigned)(lane * 16);
        const unsigned vT = (unsigned)((lane >> 3) * 2048 + (((lane & 7) ^ (((lane >> 4) & 1) << 2)) << 4));
        const char* abase = ws + WS_O + (size_t)(tt * 4 + g) * 65536;
        const char* bbase = (const char*)wo + (size_t)g * 16 * 2048 + et * 128;
        auto dma = [&](int kt, int st) {
            const int kp = __builtin_amdgcn_readlane(kptab, kt);
            const unsigned d = lds0 + st * F_ST; const char* a = abase + (size_t)kp * 4096; const char* b = bbase + (size_t)kp * 64 * 2048;
            glds16(vT, b, d + 16384 + g * 2048); glds16(vT, b + 16384, d + 16384 + g * 2048 + 1024);
#pragma unroll
            for (int i = 0; i < 4; ++i) glds16(vL, a + i * 1024, d + g * 4096 + i * 1024);
        };
        dma(0, 0); dma(1, 1);
        wait_bar<6>(); dma(2, 2); dma(3, 3);
        int sl = 4;
        for (int kt = 1; kt < 13; ++kt) { wait_bar<12>(); dma(kt + 3, sl); sl = NX5(sl); }
        wait_bar<12>(); wait_bar<6>(); wait_bar<0>();
    } else {
        const int wm = w >> 1, wn = w & 1, r = lane & 31, hh = lane >> 5;
        const int aoff = wm * 4096 + lane * 16;
        const int q4 = (lane & 15) >> 2, p4 = lane & 3, cg = (lane >> 4) & 1, pi4 = ((p4 & 1) << 1) | (p4 >> 1);
        const int tb = 16384 + (8 * hh + q4) * 128 + ((wn ^ (q4 >> 1)) << 6) + cg * 32 + pi4 * 8;
        f32x16 acc = (f32x16){};
        bf16x8 a0[2], b0[2], a1[2], b1[2];
        auto rd = [&](bf16x8 (&fa)[2], bf16x8 (&fb)[2], int st, int s2, int i) {
            lds_cptr b = (lds_cptr)lds + st * F_ST;
            fa[i] = ld128(b + aoff + (2 * s2 + i) * 1024); fb[i] = ldtr(b + tb + (2 * s2 + i) * 2048);
        };
        a1[0] = a1[1] = b1[0] = b1[1] = (bf16x8){};
#define F_ITER(st) do { \
            bar(); \
            acc = MFMA32(b1[0], a1[0], acc); rd(a0, b0, st, 0, 0); SBAR(); \
            acc = MFMA32(b1[1], a1[1], acc); rd(a0, b0, st, 0, 1); SBAR(); \
            acc = MFMA32(b0[0], a0[0], acc); rd(a1, b1, st, 1, 0); SBAR(); \
            acc = MFMA32(b0[1], a0[1], acc); rd(a1, b1, st, 1, 1); SBAR(); \
            } while (0)
        LGKM0();
        for (int kt = 0; kt < 2; ++kt) { F_ITER(0); F_ITER(1); F_ITER(2); F_ITER(3); F_ITER(4); }
        F_ITER(0); F_ITER(1);
        const char* yp = ws + WS_Y + (((size_t)tt * 4 + wm) * 32 + et * 2 + wn) * 2048 + lane * 16;
        const u32x4 ya0 = *(const u32x4*)yp, ya1 = *(const u32x4*)(yp + 1024);
        const u32x4 yb0 = *(const u32x4*)(yp + (4u << 20)), yb1 = *(const u32x4*)(yp + (4u << 20) + 1024);
        F_ITER(2); F_ITER(3); F_ITER(4); F_ITER(0);
        acc = MFMA32(b1[0], a1[0], acc); acc = MFMA32(b1[1], a1[1], acc);
#undef F_ITER
        DIAG_MARK(1);
#pragma unroll
        for (int i = 0; i < 4; ++i) {
            acc[2 * i] += __uint_as_float(ya0[i] << 16) + __uint_as_float(yb0[i] << 16);
            acc[2 * i + 1] += __uint_as_float(ya0[i] & 0xffff0000u) + __uint_as_float(yb0[i] & 0xffff0000u);
            acc[8 + 2 * i] += __uint_as_float(ya1[i] << 16) + __uint_as_float(yb1[i] << 16);
            acc[9 + 2 * i] += __uint_as_float(ya1[i] & 0xffff0000u) + __uint_as_float(yb1[i] & 0xffff0000u);
        }
        const int row = tt * 128 + wm * 32 + r;
        u16* op = out + (size_t)row * 1024 + et * 64 + wn * 32 + hh * 8;
#pragma unroll
        for (int s = 0; s < 2; ++s) *(u32x4*)(op + s * 16) = pack8(acc, s, 1.f);
    }
    DIAG_END(2, ws);
}

extern "C" void kernel_launch(void* const* d_in, const int* in_sizes, int n_in, void* d_out, int out_size, void* d_ws, size_t ws_size,
                              hipStream_t stream) {
    const u16* x = (const u16*)d_in[0]; const u16* cr = (const u16*)d_in[1]; const u16* ci = (const u16*)d_in[2];
    const u16* hr = (const u16*)d_in[3]; const u16* hi = (const u16*)d_in[4];
    const u16* wqkv = (const u16*)d_in[6]; const u16* wo = (const u16*)d_in[7]; const u16* gw = (const u16*)d_in[8];
    char* ws = (char*)d_ws;
    (void)hipFuncSetAttribute((const void*)k_proj, hipFuncAttributeMaxDynamicSharedMemorySize, P_LDS);
    (void)hipFuncSetAttribute((const void*)k_mid, hipFuncAttributeMaxDynamicSharedMemorySize, M_LDS);
    (void)hipFuncSetAttribute((const void*)k_out, hipFuncAttributeMaxDynamicSharedMemorySize, F_LDS);
    k_proj<<<256, 512, P_LDS, stream>>>(x, wqkv, hr, hi, gw, ws);
    k_mid<<<256, 1024, M_LDS, stream>>>(cr, ci, wo, ws);
    k_out<<<256, 768, F_LDS, stream>>>(ws, wo, (u16*)d_out);
}
```

```cpp
#include <hip/hip_runtime.h>
#include <hip/hip_bf16.h>

typedef unsigned short u16;
typedef short bf16x8 __attribute__((ext_vector_type(8)));
typedef short s16x4 __attribute__((ext_vector_type(4)));
typedef float f32x16 __attribute__((ext_vector_type(16)));
typedef float f32x4 __attribute__((ext_vector_type(4)));
typedef float f32x2 __attribute__((ext_vector_type(2)));
typedef unsigned u32x4 __attribute__((ext_vector_type(4)));
#define LAS __attribute__((address_space(3)))
typedef LAS const char* lds_cptr;

constexpr size_t WS_Q = 0;
constexpr size_t WS_KV = 4u << 20;
constexpr size_t WS_AB = 12u << 20;
constexpr size_t WS_O = 20u << 20;
constexpr size_t WS_Y = 24u << 20;
constexpr size_t WS_G = 40u << 20;
constexpr size_t WS_F = WS_G + 65536;
constexpr size_t WS_E = WS_F + 16384;

#define DIAG_BEGIN()
#define DIAG_MARK(i)
#define DIAG_END(kid, wsp)

#define MFMA32(a, b, c) __builtin_amdgcn_mfma_f32_32x32x16_bf16((a), (b), (c), 0, 0, 0)
#define MFMA16(a, b, c) __builtin_amdgcn_mfma_f32_16x16x32_bf16((a), (b), (c), 0, 0, 0)
#define SBAR() __builtin_amdgcn_sched_barrier(0)
#define PIN(x) asm volatile("" : "+v"(x))
#define EX2(v) __builtin_amdgcn_exp2f(v)
#define PB(p) __builtin_bit_cast(bf16x8, p)
#define LGKM0() __builtin_amdgcn_s_waitcnt(0xC07F)

__device__ __forceinline__ unsigned cvtpk(float lo, float hi) {
    typedef __bf16 bf16x2_t __attribute__((ext_vector_type(2)));
    return __builtin_bit_cast(unsigned, __builtin_convertvector((f32x2){lo, hi}, bf16x2_t)); }
__device__ __forceinline__ float bfr(float f) { return __uint_as_float(cvtpk(f, 0.f) << 16); }

__device__ __forceinline__ void glds16(unsigned voff, const void* sbase, unsigned ldsdst) {
    asm volatile("s_mov_b32 m0, %2\n\ts_nop 0\n\tglobal_load_lds_dwordx4 %0, %1" :: "v"(voff), "s"(sbase), "s"(ldsdst) : "memory");
}
#define DSC " sc1"
__device__ __forceinline__ void glds16d(unsigned voff, const void* sbase, unsigned ldsdst) {
    asm volatile("s_mov_b32 m0, %2\n\ts_nop 0\n\tglobal_load_lds_dwordx4 %0, %1" DSC :: "v"(voff), "s"(sbase), "s"(ldsdst) : "memory");
}
__device__ __forceinline__ void glds2d(unsigned v0, unsigned v1, const void* sbase, unsigned ldsdst) {
    asm volatile("s_mov_b32 m0, %3\n\ts_nop 0\n\tglobal_load_lds_dwordx4 %0, %2" DSC "\n\tglobal_load_lds_dwordx4 %1, %2 offset:1024" DSC
                 :: "v"(v0), "v"(v1), "s"(sbase), "s"(ldsdst) : "memory");
}
__device__ __forceinline__ void glds4d(unsigned v0, unsigned v1, unsigned v2, unsigned v3, const void* sbase, unsigned ldsdst) {
    asm volatile("s_mov_b32 m0, %5\n\ts_nop 0\n\tglobal_load_lds_dwordx4 %0, %4" DSC "\n\tglobal_load_lds_dwordx4 %1, %4 offset:1024" DSC "\n\t"
                 "global_load_lds_dwordx4 %2, %4 offset:2048" DSC "\n\tglobal_load_lds_dwordx4 %3, %4 offset:3072" DSC
                 :: "v"(v0), "v"(v1), "v"(v2), "v"(v3), "s"(sbase), "s"(ldsdst) : "memory");
}
__device__ __forceinline__ void glds2(unsigned v0, unsigned v1, const void* sbase, unsigned ldsdst) {
    asm volatile("s_mov_b32 m0, %3\n\ts_nop 0\n\tglobal_load_lds_dwordx4 %0, %2\n\tglobal_load_lds_dwordx4 %1, %2 offset:1024"
                 :: "v"(v0), "v"(v1), "s"(sbase), "s"(ldsdst) : "memory");
}
__device__ __forceinline__ void glds4(unsigned v0, unsigned v1, unsigned v2, unsigned v3, const void* sbase, unsigned ldsdst) {
    asm volatile("s_mov_b32 m0, %5\n\ts_nop 0\n\tglobal_load_lds_dwordx4 %0, %4\n\tglobal_load_lds_dwordx4 %1, %4 offset:1024\n\t"
                 "global_load_lds_dwordx4 %2, %4 offset:2048\n\tglobal_load_lds_dwordx4 %3, %4 offset:3072"
                 :: "v"(v0), "v"(v1), "v"(v2), "v"(v3), "s"(sbase), "s"(ldsdst) : "memory");
}
template <int N> __device__ __forceinline__ void wait_bar() {
    SBAR();
    asm volatile("s_waitcnt vmcnt(%0)\n\ts_barrier" :: "n"(N) : "memory");
    SBAR();
}
__device__ __forceinline__ void bar() { SBAR(); asm volatile("s_barrier" ::: "memory"); SBAR(); }
__device__ __forceinline__ void bar_l1() {
    SBAR();
    asm volatile("s_waitcnt lgkmcnt(1)\n\ts_barrier" ::: "memory");
    SBAR();
}
__device__ __forceinline__ bf16x8 ld128(lds_cptr p) { return *(const LAS bf16x8*)p; }
__device__ __forceinline__ bf16x8 ldtr(lds_cptr p) {
    s16x4 lo = __builtin_amdgcn_ds_read_tr16_b64_v4i16((LAS s16x4*)p);
    s16x4 hi = __builtin_amdgcn_ds_read_tr16_b64_v4i16((LAS s16x4*)(p + 512));
    return (bf16x8){lo[0], lo[1], lo[2], lo[3], hi[0], hi[1], hi[2], hi[3]};
}
__device__ __forceinline__ u32x4 pack8(const f32x16& a, int s, float sc) {
    u32x4 w;
    w[0] = cvtpk(a[8 * s + 0] * sc, a[8 * s + 1] * sc); w[1] = cvtpk(a[8 * s + 2] * sc, a[8 * s + 3] * sc);
    w[2] = cvtpk(a[8 * s + 4] * sc, a[8 * s + 5] * sc); w[3] = cvtpk(a[8 * s + 6] * sc, a[8 * s + 7] * sc);
    return w;
}
#define STF " sc1"
#define ST16(p, v) do { const u32x4 v_ = (v); asm volatile("global_store_dwordx4 %0, %1, off" STF "\n\ts_nop 1" :: "v"(p), "v"(v_) : "memory"); } while (0)
__device__ __forceinline__ int swap23(int i) { return (i & ~12) | ((i & 4) << 1) | ((i & 8) >> 1); }

__device__ __forceinline__ int lf_order(int tau, int a, int c, int G) {
    if (tau == 0) return a;
    const int g = (tau - 1) >> 2, pos = (tau - 1) & 3;
    if (g != G - 1 && pos == c) return 4 * (g + 1) + a;
    const int b = (g == G - 1 || pos < c) ? pos : pos - 1;
    return 4 * g + (b < a ? b : b + 1);
}
#define NX3(v) ((v) == 2 ? 0 : (v) + 1)
#define NX5(v) ((v) == 4 ? 0 : (v) + 1)

__device__ __forceinline__ int rswz(int row) { return ((row >> 1) & 1) | (((row >> 3) & 3) << 1); }
__device__ __forceinline__ u32x4 pack44(const f32x4& a, const f32x4& b, float sc) {
    return (u32x4){cvtpk(a[0] * sc, a[1] * sc), cvtpk(a[2] * sc, a[3] * sc), cvtpk(b[0] * sc, b[1] * sc), cvtpk(b[2] * sc, b[3] * sc)};
}
__device__ __forceinline__ void drain() { asm volatile("s_waitcnt vmcnt(0)" ::: "memory"); }
typedef unsigned long long u64;
__device__ __forceinline__ u64 ld_tag(const char* p) { return __hip_atomic_load((const u64*)p, __ATOMIC_RELAXED, __HIP_MEMORY_SCOPE_AGENT); }
__device__ __forceinline__ void st_tag(char* p, u64 v) { __hip_atomic_store((u64*)p, v, __ATOMIC_RELAXED, __HIP_MEMORY_SCOPE_AGENT); }
__device__ __forceinline__ void tags_acquire(const char* p, u64 v, u64 tag) {
#pragma nounroll
    for (int spin = 0; !__all(v == tag) && spin < (1 << 22); ++spin) { __builtin_amdgcn_s_sleep(2); v = ld_tag(p); }
    asm volatile("" ::: "memory");
}
__device__ __forceinline__ u64 call_tag(const char* ws) {
    const u64 e = ld_tag(ws + WS_E) + 1;
    return ((u64)(unsigned)__builtin_amdgcn_readfirstlane((int)(e >> 32)) << 32) | (unsigned)__builtin_amdgcn_readfirstlane((int)e);
}
__device__ __forceinline__ void bars(int n) { for (int i = 0; i < n; ++i) bar(); }

constexpr int M_KS = 8192, M_G0 = 4 * M_KS, M_GS = 32768, M_H = 16384, M_LDS = M_G0 + 4 * M_GS;
constexpr int NA = 32, NY = 44, NO = 76;
__global__ __launch_bounds__(1024, 4) void k_all(const u16* __restrict__ x, const u16* __restrict__ cr, const u16* __restrict__ ci,
                                                  const u16* __restrict__ hr, const u16* __restrict__ hi, const u16* __restrict__ wqkv,
                                                  const u16* __restrict__ wo, const u16* __restrict__ gw, char* ws, u16* __restrict__ out) {
    extern __shared__ __attribute__((aligned(16))) char smem[];
    LAS char* lds = (LAS char*)smem;
    DIAG_BEGIN();
    const int tid = threadIdx.x, lane = tid & 63, w = __builtin_amdgcn_readfirstlane(tid >> 6);
    const int xcd = blockIdx.x & 7, idx = blockIdx.x >> 3;
    const int rt = (xcd & 1) * 8 + (idx & 7), j = (xcd >> 1) * 4 + (idx >> 3);
    const int head = xcd * 4 + (idx >> 3), qb = idx & 7;
    const int part = xcd >> 2, tt = ((xcd >> 1) & 1) * 8 + (idx & 7), et = (xcd & 1) * 4 + (idx >> 3);
    const int r = lane & 31, hh = lane >> 5, c = lane & 15, g4 = lane >> 4;
    const int sg0 = 8 * (c >> 2) + (c & 3);
    const char* kb = ws + WS_KV + (size_t)head * 131072;
    lds_cptr lp = (lds_cptr)lds + lane * 16;
    char* myflag = ws + WS_F + blockIdx.x * 8;
    if (w >= 12) {
        const int g = w - 12;
        const int la = ((idx >> 3) - (idx & 7)) & 3, lc = (idx >> 1) & 3;
        const int tab16 = lf_order(lane & 15, la, lc, 4), tab32 = lf_order(lane & 31, la, lc, 8);
        const unsigned lds0 = (unsigned)(uintptr_t)lds;
        const unsigned vL = (unsigned)(lane * 16);
        unsigned vR[4], vW[2], vC[4], vA[2], vT[2];
#pragma unroll
        for (int i = 0; i < 4; ++i) {
            vR[i] = (unsigned)((8 * i + (lane >> 3)) * 2048 + (((lane & 7) ^ rswz(8 * i + (lane >> 3))) << 4) - i * 1024);
            vC[i] = (unsigned)((8 * i + (lane >> 3)) * 4096 + (((lane & 7) ^ ((4 * i + (lane >> 4)) & 7)) << 4) - i * 1024);
        }
#pragma unroll
        for (int i = 0; i < 2; ++i) {
            vW[i] = (unsigned)((8 * i + (lane >> 3)) * 6144 + (((lane & 7) ^ (((lane >> 4) & 1) << 2) ^ (i << 1)) << 4) - i * 1024);
            vT[i] = (unsigned)((8 * i + (lane >> 3)) * 2048 + (((lane & 7) ^ (((lane >> 4) & 1) << 2)) << 4) - i * 1024);
            vA[i] = (unsigned)(lane * 16 + i * (65536 - 2048));
        }
        const unsigned vKV = (unsigned)(lane * 16 + (4 << 20) - 3072 - 1024);
        const char* xb = (const char*)x + (size_t)(rt * 128 + g * 32) * 2048;
        const char* hb = (const char*)(j < 8 ? hr : hi) + (size_t)((j & 7) * 128 + g * 32) * 2048;
        const char* wb = (const char*)wqkv + (size_t)g * 16 * 6144 + j * 128;
        const char* cb = (const char*)(part ? ci : cr) + (size_t)(tt * 128 + 32 * g) * 4096;
        const char* ab = ws + WS_AB + (size_t)(part * 64 + et * 8 + 2 * g) * 65536;
        const char* ob = ws + WS_O + (size_t)(tt * 4 + g) * 65536;
        const char* wob = (const char*)wo + (2 * et + part) * 128;
        const unsigned dG = lds0 + M_G0 + g * 4096, dK = lds0 + g * 2048;
        auto sP = [&](int n) {
            const int kp = __builtin_amdgcn_readlane(tab16, n); const unsigned so = (n & 3) * M_GS;
            const char* b = wb + (size_t)(kp * (64 * 6144));
            glds4(vR[0], vR[1], vR[2], vR[3], xb + kp * 128, dG + so);
            glds2(vW[0], vW[1], b, dG + so + M_H - g * 2048); glds2(vW[0], vW[1], b + 2048, dG + so + M_H + 8192 - g * 2048);
            glds2(vW[0], vW[1], b + 4096, dK + (n & 3) * M_KS);
        };
        auto sH = [&](int n) {
            const int kp = __builtin_amdgcn_readlane(tab16, n & 15); const unsigned so = (n & 3) * M_GS;
            glds4(vR[0], vR[1], vR[2], vR[3], xb + kp * 128, dG + so); glds4(vR[0], vR[1], vR[2], vR[3], hb + kp * 128, dG + so + M_H);
        };
        auto sKV = [&](int n) {
            glds2d(vL, vKV, kb + ((n - NA) * 4096 + 4096 + g * 1024), dK + (n & 3) * M_KS);
        };
        auto sY = [&](int n) {
            const int kp = __builtin_amdgcn_readlane(tab32, n - NY); const unsigned so = (n & 3) * M_GS;
            glds4(vC[0], vC[1], vC[2], vC[3], cb + kp * 128, dG + so); glds4d(vA[0], vA[0], vA[1], vA[1], ab + kp * 2048, dG + so + M_H);
        };
        auto sO = [&](int n) {
            const int kp = __builtin_amdgcn_readlane(tab16, n - NO); const unsigned so = (n & 3) * M_GS;
            glds2(vT[0], vT[1], wob + (size_t)((kp * 64 + g * 16) * 2048), dG + so + M_H - g * 2048);
            glds4d(vL, vL, vL, vL, ob + kp * 4096, dG + so);
        };
#define STAMP()
#define STAMP_DUMP()
        sP(0);
        for (int m = 1; m < 16; ++m) { sP(m); wait_bar<10>(); STAMP(); }
        for (int m = 16; m < 31; ++m) { sH(m); wait_bar<8>(); STAMP(); }
        glds16d(vL, kb + g * 1024, dK + 3 * M_KS); glds16d(vL, kb + (4 << 20), dK + 3 * M_KS + 1024);
        sH(31); wait_bar<10>(); STAMP();
        auto sC = [&](int n) { const int kp = __builtin_amdgcn_readlane(tab32, n - NY); glds4(vC[0], vC[1], vC[2], vC[3], cb + kp * 128, dG + (n & 3) * M_GS); };
        auto sAB = [&](int n) { const int kp = __builtin_amdgcn_readlane(tab32, n - NY); glds4d(vA[0], vA[0], vA[1], vA[1], ab + kp * 2048, dG + (n & 3) * M_GS + M_H); };
        for (int m = NA; m < NA + 4; ++m) { sKV(m); wait_bar<2>(); STAMP(); }
        sKV(NA + 4); glds4(vR[0], vR[1], vR[2], vR[3], wob + (size_t)(((idx & 7) * 128 + g * 32) * 2048), dG + M_H); wait_bar<6>(); STAMP();
        sKV(NA + 5); wait_bar<2>(); STAMP();
        sKV(NA + 6); wait_bar<2>(); STAMP();
        for (int m = NA + 7; m < NA + 11; ++m) { sKV(m); sC(m + 5); wait_bar<6>(); STAMP(); }
        sKV(NA + 11); wait_bar<2>(); STAMP();
        for (int m = NY; m < NY + 4; ++m) { sKV(m); sAB(m); wait_bar<6>(); STAMP(); }
        for (int m = NY + 4; m < NA + 31; ++m) { sKV(m); sY(m); wait_bar<10>(); STAMP(); }
        { const int vo = g < 3 ? 31 * 4096 + 1024 + g * 1024 : 0;
          glds16d(vL, kb + (4 << 20) + vo, dK + 3 * M_KS + 1024); sY(NA + 31); wait_bar<9>(); STAMP(); }
        for (int m = NA + 32; m < NO - 4; ++m) { sY(m); wait_bar<8>(); STAMP(); }
        glds16d(vL, ws + WS_G + (size_t)tt * 1024, lds0 + 16384);
        sY(NO - 4); wait_bar<9>(); STAMP();
        for (int m = NO - 3; m < NO; ++m) { sY(m); wait_bar<8>(); STAMP(); }
        for (int m = NO; m < NO + 14; ++m) { sO(m); wait_bar<6>(); STAMP(); }
        sO(NO + 14); glds4d(vL, vL, vL, vL, ws + WS_Y + (size_t)(blockIdx.x ^ 4) * 16384 + g * 4096, lds0 + 16384 + g * 4096);
        wait_bar<10>(); STAMP();
        sO(NO + 15); wait_bar<6>(); STAMP();
        wait_bar<0>(); STAMP();
        STAMP_DUMP();
    } else if (w >= 8) {
        const int g = w - 8, gm = g >> 1, gn = g & 1;
        const u64 tag = call_tag(ws);
        __builtin_amdgcn_s_setprio(3);
        bars(2);
        {
            const u32x4* xr = (const u32x4*)(x + (size_t)(blockIdx.x * 8 + 2 * g) * 1024); const u32x4* gr = (const u32x4*)gw;
            const u32x4 a0 = xr[lane], a1 = xr[lane + 64], a2 = xr[lane + 128], a3 = xr[lane + 192], b0 = gr[lane], b1 = gr[lane + 64];
            bars(3);
            float z0 = 0.f, z1 = 0.f;
#pragma unroll
            for (int e = 0; e < 4; ++e) {
                z0 = fmaf(__uint_as_float(a0[e] << 16), __uint_as_float(b0[e] << 16), z0); z0 = fmaf(__uint_as_float(a0[e] & 0xffff0000u), __uint_as_float(b0[e] & 0xffff0000u), z0);
                z0 = fmaf(__uint_as_float(a1[e] << 16), __uint_as_float(b1[e] << 16), z0); z0 = fmaf(__uint_as_float(a1[e] & 0xffff0000u), __uint_as_float(b1[e] & 0xffff0000u), z0);
            }
            PIN(z0);
            bar();
#pragma unroll
            for (int e = 0; e < 4; ++e) {
                z1 = fmaf(__uint_as_float(a2[e] << 16), __uint_as_float(b0[e] << 16), z1); z1 = fmaf(__uint_as_float(a2[e] & 0xffff0000u), __uint_as_float(b0[e] & 0xffff0000u), z1);
                z1 = fmaf(__uint_as_float(a3[e] << 16), __uint_as_float(b1[e] << 16), z1); z1 = fmaf(__uint_as_float(a3[e] & 0xffff0000u), __uint_as_float(b1[e] & 0xffff0000u), z1);
            }
            PIN(z1);
#pragma unroll
            for (int o = 32; o >= 1; o >>= 1) { bar(); z0 += __shfl_xor(z0, o); z1 += __shfl_xor(z1, o); PIN(z0); PIN(z1); }
            bar();
            const float gt = bfr(1.f / (1.f + __expf(-bfr((lane & 1) ? z1 : z0))));
            if (lane < 2) st_tag(ws + WS_G + (size_t)(blockIdx.x * 8 + 2 * g + lane) * 8, ((u64)__float_as_uint(bfr(1.f - gt)) << 32) | __float_as_uint(gt));
            __builtin_amdgcn_s_setprio(0);
            bars(2);
        }
        f32x4 y[4][4];
        bf16x8 cA[4], cB[4], fa[4];
        lds_cptr ca[2][2], fp[2][2];
#define G_MM(SW, a_, CX, m_) y[a_][m_] = (SW) ? MFMA16(CX[m_], fa[a_], y[a_][m_]) : MFMA16(fa[a_], CX[m_], y[a_][m_])
#define G_STEP(SW, CP, CC, ST, HF, CO, FO) do { \
            _Pragma("unroll") for (int s_ = 0; s_ < 4; ++s_) { \
                _Pragma("unroll") for (int m = 0; m < 4; ++m) { \
                    if (s_ < 2) G_MM(SW, s_ + 2, CP, m); else G_MM(SW, s_ - 2, CC, m); \
                    if (s_ == 0) CC[m] = ld128(ca[(ST) >> 1][HF] + ((ST) & 1) * M_GS + CO(m)); \
                    if (m == 0) fa[s_] = ld128(fp[(ST) >> 1][HF] + ((ST) & 1) * M_GS + FO(s_)); \
                    SBAR(); } } } while (0)
#define G_ITER(SW, st, CO, FO) do { bar(); G_STEP(SW, cB, cA, st, 0, CO, FO); G_STEP(SW, cA, cB, st, 1, CO, FO); } while (0)
#define G_RUN(SW, NIT, CO, FO, PRE) do { \
            _Pragma("unroll") for (int a = 0; a < 4; ++a) { cB[a] = (bf16x8){}; fa[a] = (bf16x8){}; \
                _Pragma("unroll") for (int b = 0; b < 4; ++b) y[a][b] = (f32x4){}; } \
            PRE; LGKM0(); \
            for (int it = 0; it < NIT; ++it) { G_ITER(SW, 0, CO, FO); G_ITER(SW, 1, CO, FO); G_ITER(SW, 2, CO, FO); G_ITER(SW, 3, CO, FO); } \
            bar(); \
            _Pragma("unroll") for (int a = 2; a < 4; ++a) _Pragma("unroll") for (int m = 0; m < 4; ++m) G_MM(SW, a, cB, m); } while (0)
#define OFF_SIG(i) (((i) >> 1) * 4096 + ((i) & 1) * 512)
#define OFF_2K(i) ((i) * 2048)
#pragma unroll
        for (int h = 0; h < 2; ++h)
#pragma unroll
            for (int hf = 0; hf < 2; ++hf) {
                const int ra = M_G0 + h * 2 * M_GS + sg0 * 128 + (((4 * hf + g4) ^ rswz(sg0)) << 4);
                ca[h][hf] = (lds_cptr)lds + ra + gm * 8192; fp[h][hf] = (lds_cptr)lds + ra + M_H + gn * 8192; PIN(ca[h][hf]); PIN(fp[h][hf]);
            }
        G_RUN(true, 4, OFF_SIG, OFF_SIG, drain(); bar());
#pragma unroll
        for (int gr = 0; gr < 2; ++gr) {
            char* abp = ws + WS_AB + ((size_t)(j * 8 + gn * 4) * 64 + rt * 4 + gm * 2 + gr) * 1024 + lane * 16;
#pragma unroll
            for (int b = 0; b < 4; ++b) ST16(abp + (size_t)b * 65536, pack44(y[b][2 * gr], y[b][2 * gr + 1], 1.f));
            bar();
        }
        bar();
        drain();
        bar();
        if (g == 0) {
            if (lane == 0) st_tag(myflag + 2048, tag);
            const int jj = part * 8 + et, rr = lane & 15;
            const char* fpn = ws + WS_F + 2048 + (((rr & 7) | ((jj & 3) << 3)) * 8 + ((rr >> 3) | ((jj >> 2) << 1))) * 8;
            bars(4);
            const u64 v = ld_tag(fpn);
            bar();
            tags_acquire(fpn, v, tag);
        } else bars(5);
        bar();
#pragma unroll
        for (int h = 0; h < 2; ++h)
#pragma unroll
            for (int hf = 0; hf < 2; ++hf) {
                ca[h][hf] = (lds_cptr)lds + M_G0 + h * 2 * M_GS + (gm * 64 + c) * 128 + (((4 * hf + g4) ^ ((c >> 1) & 7)) << 4);
                fp[h][hf] = lp + M_G0 + h * 2 * M_GS + M_H + gn * 8192 + hf * 1024; PIN(ca[h][hf]); PIN(fp[h][hf]);
            }
        G_RUN(false, 8, OFF_2K, OFF_2K, bar());
#pragma unroll
        for (int m = 0; m < 4; ++m) {
            float gy = *(const LAS float*)(lds + 16384 + (gm * 64 + m * 16 + c) * 8);
            if (part) gy = -gy;
#pragma unroll
            for (int e = 0; e < 2; ++e) {
                const int off = ((gm * 2 + (m >> 1)) * 2 + e) * 2048 + (g4 >> 1) * 1024 + ((g4 & 1) * 32 + (m & 1) * 16 + c) * 16;
                const u32x4 v = pack44(y[2 * e][m], y[2 * e + 1][m], gy);
                if (gn == part) *(LAS u32x4*)(lds + off) = v; else ST16(ws + WS_Y + (size_t)blockIdx.x * 16384 + off, v);
            }
            bar();
        }
        bars(2);
        asm volatile("s_waitcnt vmcnt(0) lgkmcnt(0)" ::: "memory");
        bar();
        if (g == 0) {
            if (lane == 0) st_tag(myflag + 6144, tag);
            const char* fpn = ws + WS_F + 6144 + (blockIdx.x ^ 4) * 8;
            bars(3);
            const u64 v = ld_tag(fpn);
            bar();
            tags_acquire(fpn, v, tag);
            bar();
        }
    } else {
        const u64 tag = call_tag(ws);
        {
            const int wm = w >> 1, wn = w & 1;
            lds_cptr xa[2][2], tq[2][2], tv[2];
            { const int q4 = c >> 2, p4 = c & 3;
#pragma unroll
              for (int i = 0; i < 2; ++i) {
                  const int tbo = (8 * g4 + q4) * 128 + ((((wn * 4 + i * 2) ^ ((q4 >> 1) << 2) ^ ((g4 & 1) << 1)) + (p4 >> 1)) << 4) + (p4 & 1) * 8;
                  tv[i] = (lds_cptr)lds + tbo; PIN(tv[i]);
#pragma unroll
                  for (int h = 0; h < 2; ++h) {
                      xa[h][i] = (lds_cptr)lds + M_G0 + h * 2 * M_GS + wm * 4096 + sg0 * 128 + (((4 * i + g4) ^ rswz(sg0)) << 4); PIN(xa[h][i]);
                      tq[h][i] = (lds_cptr)lds + M_G0 + h * 2 * M_GS + M_H + tbo; PIN(tq[h][i]);
                  } } }
            f32x4 acc[6][2];
            bf16x8 xA[2], xB[2], fb[6];
#pragma unroll
            for (int b = 0; b < 6; ++b) { acc[b][0] = (f32x4){}; acc[b][1] = (f32x4){}; }
            xB[0] = xB[1] = fb[3] = fb[4] = fb[5] = (bf16x8){};
#define Q_MM(b_, XF, m_) acc[b_][m_] = (b_) < 4 ? MFMA16(fb[b_], XF[m_], acc[b_][m_]) : MFMA16(XF[m_], fb[b_], acc[b_][m_])
#define Q_STEP(XP, XC, ST, HF) do { \
                _Pragma("unroll") for (int s_ = 0; s_ < 6; ++s_) { \
                    _Pragma("unroll") for (int m = 0; m < 2; ++m) { \
                        if (s_ < 3) Q_MM(s_ + 3, XP, m); else Q_MM(s_ - 3, XC, m); \
                        SBAR(); \
                        if (s_ == 0) XC[m] = ld128(xa[(ST) >> 1][HF] + ((ST) & 1) * M_GS + m * 512); \
                        { const s16x4 v_ = __builtin_amdgcn_ds_read_tr16_b64_v4i16((LAS s16x4*)(m * 512 + (HF) * 4096 + \
                              (s_ < 4 ? tq[(ST) >> 1][s_ & 1] + ((ST) & 1) * M_GS + (s_ >> 1) * 8192 : tv[s_ & 1] + (ST) * M_KS))); \
                          _Pragma("unroll") for (int e_ = 0; e_ < 4; ++e_) fb[s_][4 * m + e_] = v_[e_]; } \
                        SBAR(); } } } while (0)
#define Q_ITER(st) do { bar(); Q_STEP(xB, xA, st, 0); Q_STEP(xA, xB, st, 1); } while (0)
            LGKM0();
            for (int it = 0; it < 4; ++it) { Q_ITER(0); Q_ITER(1); Q_ITER(2); Q_ITER(3); }
            bar();
#pragma unroll
            for (int b = 3; b < 6; ++b) { Q_MM(b, xB, 0); Q_MM(b, xB, 1); }
            DIAG_MARK(0);
            const int hd = 2 * j + wn, t32 = rt * 4 + wm;
            const float qs = 0.17677669529663687f * 1.4426950408889634f;
            const int fo = (g4 >> 1) * 1024 + (g4 & 1) * 512;
            char* qp = ws + WS_Q + (size_t)(hd * 64 + t32) * 2048 + fo;
            char* kp = ws + WS_KV + (size_t)(hd * 64 + t32) * 2048 + fo;
#pragma unroll
            for (int a = 0; a < 2; ++a) { ST16(kp + (16 * (c >> 3) + 8 * a + (c & 7)) * 16, pack44(acc[2][a], acc[3][a], 1.f)); bar(); }
#pragma unroll
            for (int a = 0; a < 2; ++a) { ST16(kp + (4u << 20) + (16 * a + swap23(c)) * 16, pack44(acc[4 + a][0], acc[4 + a][1], 1.f)); bar(); }
#pragma unroll
            for (int a = 0; a < 2; ++a) { ST16(qp + (sg0 + 4 * a) * 16, pack44(acc[0][a], acc[1][a], qs)); bar(); }
        }
        bar();
        drain();
        bar();
        if (w == 0) {
            if (lane == 0) st_tag(myflag, tag);
            const int jj = head >> 1, rr = lane & 15;
            const int pb = lane < 16 ? ((rr & 7) | ((jj & 3) << 3)) * 8 + ((rr >> 3) | ((jj >> 2) << 1)) : (lane < 32 ? tt * 16 + rr : qb * 32 + (lane - 32));
            const char* fpn = ws + WS_F + pb * 8;
            bars(3);
            const u64 v = ld_tag(fpn);
            bar();
            tags_acquire(fpn, v, tag);
        } else bars(4);
        bar();
        const int qt = qb * 8 + w;
        bf16x8 qf[2]; float omg;
        { const char* qp = ws + WS_Q + (size_t)(head * 64 + qt) * 2048 + lane * 16;
          asm volatile("global_load_dwordx4 %0, %1, off sc1" : "=v"(qf[0]) : "v"(qp) : "memory");
          asm volatile("global_load_dwordx4 %0, %1, off offset:1024 sc1" : "=v"(qf[1]) : "v"(qp) : "memory");
          asm volatile("global_load_dword %0, %1, off offset:4 sc1" : "=v"(omg) : "v"(ws + WS_G + (size_t)(qt * 32 + r) * 8) : "memory"); }
        bf16x8 sel; { const short one = (((lane & 1) == ((lane >> 4) & 1))) ? (short)0x3f80 : (short)0;
#pragma unroll
            for (int i = 0; i < 8; ++i) sel[i] = one; }
        f32x16 o = (f32x16){}; f32x4 l4 = (f32x4){};
        f32x16 negm, sA, sB;
        bf16x8 kf[2], vf0, vf1;
        u32x4 p0, p1;
        bars(2);
        asm volatile("s_waitcnt vmcnt(0)" : "+v"(qf[0]), "+v"(qf[1]), "+v"(omg) :: "memory");
        DIAG_MARK(1);
        kf[0] = ld128(lp + 3 * M_KS); kf[1] = ld128(lp + 3 * M_KS + 2048);
        sA = MFMA32(kf[0], qf[0], (f32x16){}); sA = MFMA32(kf[1], qf[1], sA);
        { float m = sA[0];
#pragma unroll
          for (int i = 1; i < 16; ++i) m = fmaxf(m, sA[i]);
          m = fmaxf(m, __shfl_xor(m, 32));
#pragma unroll
          for (int i = 0; i < 16; ++i) { negm[i] = -m; sA[i] -= m; } }
        kf[0] = ld128(lp + 3 * M_KS + 4096); kf[1] = ld128(lp + 3 * M_KS + 6144);
        vf0 = ld128(lp + 3 * M_KS + 1024);
        vf1 = vf0; p1 = (u32x4){0u, 0u, 0u, 0u};
#define EN(S, i, n) do { _Pragma("unroll") for (int e_ = 0; e_ < n; ++e_) S[i + e_] = EX2(S[i + e_]); } while (0)
#define A_STEP(SC, SN, v1o, kn, v0n) do { \
            SN = MFMA32(kf[0], qf[0], negm); PIN(SN); SBAR(); \
            EN(SC, 0, 3); PIN(SC); SBAR(); \
            o = MFMA32(vf1, PB(p1), o); PIN(o); SBAR(); \
            EN(SC, 3, 3); PIN(SC); SBAR(); \
            SN = MFMA32(kf[1], qf[1], SN); PIN(SN); SBAR(); \
            vf1 = ld128(lp + (v1o)); kf[0] = ld128(lp + (kn)); kf[1] = ld128(lp + (kn) + 2048); SBAR(); \
            EN(SC, 6, 2); p0[0] = cvtpk(SC[0], SC[1]); p0[1] = cvtpk(SC[2], SC[3]); PIN(SC); PIN(p0); SBAR(); \
            l4 = MFMA16(sel, PB(p1), l4); PIN(l4); SBAR(); \
            p0[2] = cvtpk(SC[4], SC[5]); p0[3] = cvtpk(SC[6], SC[7]); PIN(p0); SBAR(); EN(SC, 8, 2); PIN(SC); SBAR(); \
            o = MFMA32(vf0, PB(p0), o); PIN(o); SBAR(); \
            vf0 = ld128(lp + (v0n)); SBAR(); \
            EN(SC, 10, 3); PIN(SC); SBAR(); \
            l4 = MFMA16(sel, PB(p0), l4); PIN(l4); SBAR(); \
            EN(SC, 13, 3); \
            _Pragma("unroll") for (int i = 0; i < 4; ++i) p1[i] = cvtpk(SC[8 + 2 * i], SC[9 + 2 * i]); \
            PIN(p1); SBAR(); \
        } while (0)
#define A_ITER(st) do { \
            constexpr int b0 = (st) * M_KS; \
            bar(); \
            A_STEP(sA, sB, b0 + 1024, b0, b0 + 3072); \
            A_STEP(sB, sA, b0 + 5120, b0 + 4096, b0 + 7168); \
            } while (0)
        static_assert(NA % 4 == 0 && NY % 4 == 0 && NO % 4 == 0, "slot phase");
        LGKM0();
        for (int it = 0; it < 8; ++it) { A_ITER(0); A_ITER(1); A_ITER(2); A_ITER(3); }
        bar();
        o = MFMA32(vf1, PB(p1), o); l4 = MFMA16(sel, PB(p1), l4);
        DIAG_MARK(2);
        float l = ((lane >> 4) & 1) ? l4[1] : l4[0];
        bool ok = l < 1.0e30f;
#pragma unroll
        for (int i = 0; i < 16; ++i) ok = ok && (fabsf(o[i]) < 3.0e38f);
        if (!__all(ok)) {
            float m = -INFINITY, ls = 0.f; o = (f32x16){};
            const char* gp = kb + lane * 16;
            for (int t = 0; t < 64; ++t) {
                bf16x8 k0, k1, v0, v1;
                asm volatile("global_load_dwordx4 %0, %4, off sc1\n\tglobal_load_dwordx4 %1, %4, off offset:1024 sc1\n\t"
                             "global_load_dwordx4 %2, %5, off sc1\n\tglobal_load_dwordx4 %3, %5, off offset:1024 sc1\n\ts_waitcnt vmcnt(0)"
                             : "=&v"(k0), "=&v"(k1), "=&v"(v0), "=&v"(v1) : "v"(gp + t * 2048), "v"(gp + (4 << 20) + t * 2048) : "memory");
                f32x16 s = MFMA32(k0, qf[0], (f32x16){}); s = MFMA32(k1, qf[1], s);
                float tm = s[0];
#pragma unroll
                for (int i = 1; i < 16; ++i) tm = fmaxf(tm, s[i]);
                tm = fmaxf(tm, __shfl_xor(tm, 32));
                const float mn = fmaxf(m, tm), al = EX2(m - mn);
                m = mn; ls *= al;
#pragma unroll
                for (int i = 0; i < 16; ++i) { o[i] *= al; s[i] = EX2(s[i] - mn); ls += s[i]; }
                u32x4 a0, a1;
#pragma unroll
                for (int i = 0; i < 4; ++i) { a0[i] = cvtpk(s[2 * i], s[2 * i + 1]); a1[i] = cvtpk(s[8 + 2 * i], s[9 + 2 * i]); }
                o = MFMA32(v0, PB(a0), o); o = MFMA32(v1, PB(a1), o);
            }
            l = ls + __shfl_xor(ls, 32);
        }
        { const float sc = omg / l;
          char* op = ws + WS_O + ((size_t)qt * 64 + 2 * head) * 1024 + lane * 16;
#pragma unroll
          for (int s = 0; s < 2; ++s) ST16(op + s * 1024, pack8(o, s, sc)); }
        bars(2);
        drain();
        bar();
        if (w == 0) {
            if (lane == 0) st_tag(myflag + 4096, tag);
            const int hd = lane & 31;
            const char* fpn = ws + WS_F + 4096 + (((hd & 3) * 8 + (tt >> 1)) * 8 + (hd >> 2)) * 8;
            bars(4);
            const u64 v = ld_tag(fpn);
            bars(2);
            tags_acquire(fpn, v, tag);
        } else bars(6);
        bars(2);
        DIAG_MARK(3);
        {
            const int wm = w >> 1, wn = w & 1;
            const int q4 = (lane & 15) >> 2, p4 = lane & 3, cg = (lane >> 4) & 1, pi4 = ((p4 & 1) << 1) | (p4 >> 1);
            lds_cptr ap[2], bp[2];
#pragma unroll
            for (int h = 0; h < 2; ++h) {
                ap[h] = lp + M_G0 + h * 2 * M_GS + wm * 4096;
                bp[h] = (lds_cptr)lds + M_G0 + h * 2 * M_GS + M_H + (8 * hh + q4) * 128 + ((wn ^ (q4 >> 1)) << 6) + cg * 32 + pi4 * 8;
            }
            f32x16 acc = (f32x16){};
            bf16x8 a0[2], b0[2], a1[2], b1[2];
            a1[0] = a1[1] = b1[0] = b1[1] = (bf16x8){};
#define F_RD(FA, FB, st, s2, i) do { FA[i] = ld128(ap[(st) >> 1] + ((st) & 1) * M_GS + (2 * (s2) + (i)) * 1024); \
                                     FB[i] = ldtr(bp[(st) >> 1] + ((st) & 1) * M_GS + (2 * (s2) + (i)) * 2048); } while (0)
#define F_ITER(st) do { \
                bar(); \
                acc = MFMA32(b1[0], a1[0], acc); F_RD(a0, b0, st, 0, 0); SBAR(); \
                acc = MFMA32(b1[1], a1[1], acc); F_RD(a0, b0, st, 0, 1); SBAR(); \
                acc = MFMA32(b0[0], a0[0], acc); F_RD(a1, b1, st, 1, 0); SBAR(); \
                acc = MFMA32(b0[1], a0[1], acc); F_RD(a1, b1, st, 1, 1); SBAR(); \
                } while (0)
            LGKM0();
            for (int it = 0; it < 4; ++it) { F_ITER(0); F_ITER(1); F_ITER(2); F_ITER(3); }
            acc = MFMA32(b1[0], a1[0], acc); acc = MFMA32(b1[1], a1[1], acc);
            DIAG_MARK(4);
#pragma unroll
            for (int s = 0; s < 2; ++s) {
                const u32x4 ya = *(const LAS u32x4*)(lp + w * 2048 + s * 1024), yb = *(const LAS u32x4*)(lp + 16384 + w * 2048 + s * 1024);
#pragma unroll
                for (int i = 0; i < 4; ++i) {
                    acc[8 * s + 2 * i] += __uint_as_float(ya[i] << 16) + __uint_as_float(yb[i] << 16);
                    acc[8 * s + 2 * i + 1] += __uint_as_float(ya[i] & 0xffff0000u) + __uint_as_float(yb[i] & 0xffff0000u);
                }
            }
            u16* op = out + (size_t)(tt * 128 + wm * 32 + r) * 1024 + (2 * et + part) * 64 + wn * 32 + hh * 8;
#pragma unroll
            for (int s = 0; s < 2; ++s) ST16(op + s * 16, pack8(acc, s, 1.f));
        }
        if (blockIdx.x == 0 && tid == 0) st_tag(ws + WS_E, tag);
        DIAG_END(0, ws);
    }
}

extern "C" void kernel_launch(void* const* d_in, const int* in_sizes, int n_in, void* d_out, int out_size, void* d_ws, size_t ws_size,
                              hipStream_t stream) {
    (void)hipFuncSetAttribute((const void*)k_all, hipFuncAttributeMaxDynamicSharedMemorySize, M_LDS);
    k_all<<<256, 1024, M_LDS, stream>>>((const u16*)d_in[0], (const u16*)d_in[1], (const u16*)d_in[2], (const u16*)d_in[3], (const u16*)d_in[4],
                                        (const u16*)d_in[6], (const u16*)d_in[7], (const u16*)d_in[8], (char*)d_ws, (u16*)d_out);
}
```

```cpp
#include <hip/hip_runtime.h>
#include <hip/hip_bf16.h>

#define WARM 0
#define RTSTAMP 0
typedef unsigned short u16;
typedef short bf16x8 __attribute__((ext_vector_type(8)));
typedef short s16x4 __attribute__((ext_vector_type(4)));
typedef float f32x16 __attribute__((ext_vector_type(16)));
typedef float f32x4 __attribute__((ext_vector_type(4)));
typedef float f32x2 __attribute__((ext_vector_type(2)));
typedef unsigned u32x4 __attribute__((ext_vector_type(4)));
#define LAS __attribute__((address_space(3)))
typedef LAS const char* lds_cptr;

constexpr size_t WS_Q = 0;
constexpr size_t WS_KV = 4u << 20;
constexpr size_t WS_AB = 12u << 20;
constexpr size_t WS_O = 20u << 20;
constexpr size_t WS_Y = 24u << 20;
constexpr size_t WS_G = 40u << 20;
constexpr size_t WS_F = WS_G + 65536;
constexpr size_t WS_E = WS_F + 16384;

#define DIAG_BEGIN()
#define DIAG_MARK(i)
#define DIAG_END(kid, wsp)

#define MFMA32(a, b, c) __builtin_amdgcn_mfma_f32_32x32x16_bf16((a), (b), (c), 0, 0, 0)
#define MFMA16(a, b, c) __builtin_amdgcn_mfma_f32_16x16x32_bf16((a), (b), (c), 0, 0, 0)
#define SBAR() __builtin_amdgcn_sched_barrier(0)
#define PIN(x) asm volatile("" : "+v"(x))
#define EX2(v) __builtin_amdgcn_exp2f(v)
#define PB(p) __builtin_bit_cast(bf16x8, p)
#define LGKM0() __builtin_amdgcn_s_waitcnt(0xC07F)

__device__ __forceinline__ unsigned cvtpk(float lo, float hi) {
    typedef __bf16 bf16x2_t __attribute__((ext_vector_type(2)));
    return __builtin_bit_cast(unsigned, __builtin_convertvector((f32x2){lo, hi}, bf16x2_t)); }
__device__ __forceinline__ float bfr(float f) { return __uint_as_float(cvtpk(f, 0.f) << 16); }

__device__ __forceinline__ void glds16(unsigned voff, const void* sbase, unsigned ldsdst) {
    asm volatile("s_mov_b32 m0, %2\n\ts_nop 0\n\tglobal_load_lds_dwordx4 %0, %1" :: "v"(voff), "s"(sbase), "s"(ldsdst) : "memory");
}
#define DSC " sc1"
__device__ __forceinline__ void glds16d(unsigned voff, const void* sbase, unsigned ldsdst) {
    asm volatile("s_mov_b32 m0, %2\n\ts_nop 0\n\tglobal_load_lds_dwordx4 %0, %1" DSC :: "v"(voff), "s"(sbase), "s"(ldsdst) : "memory");
}
__device__ __forceinline__ void glds2d(unsigned v0, unsigned v1, const void* sbase, unsigned ldsdst) {
    asm volatile("s_mov_b32 m0, %3\n\ts_nop 0\n\tglobal_load_lds_dwordx4 %0, %2" DSC "\n\tglobal_load_lds_dwordx4 %1, %2 offset:1024" DSC
                 :: "v"(v0), "v"(v1), "s"(sbase), "s"(ldsdst) : "memory");
}
__device__ __forceinline__ void glds4d(unsigned v0, unsigned v1, unsigned v2, unsigned v3, const void* sbase, unsigned ldsdst) {
    asm volatile("s_mov_b32 m0, %5\n\ts_nop 0\n\tglobal_load_lds_dwordx4 %0, %4" DSC "\n\tglobal_load_lds_dwordx4 %1, %4 offset:1024" DSC "\n\t"
                 "global_load_lds_dwordx4 %2, %4 offset:2048" DSC "\n\tglobal_load_lds_dwordx4 %3, %4 offset:3072" DSC
                 :: "v"(v0), "v"(v1), "v"(v2), "v"(v3), "s"(sbase), "s"(ldsdst) : "memory");
}
__device__ __forceinline__ void glds2(unsigned v0, unsigned v1, const void* sbase, unsigned ldsdst) {
    asm volatile("s_mov_b32 m0, %3\n\ts_nop 0\n\tglobal_load_lds_dwordx4 %0, %2\n\tglobal_load_lds_dwordx4 %1, %2 offset:1024"
                 :: "v"(v0), "v"(v1), "s"(sbase), "s"(ldsdst) : "memory");
}
__device__ __forceinline__ void glds4(unsigned v0, unsigned v1, unsigned v2, unsigned v3, const void* sbase, unsigned ldsdst) {
    asm volatile("s_mov_b32 m0, %5\n\ts_nop 0\n\tglobal_load_lds_dwordx4 %0, %4\n\tglobal_load_lds_dwordx4 %1, %4 offset:1024\n\t"
                 "global_load_lds_dwordx4 %2, %4 offset:2048\n\tglobal_load_lds_dwordx4 %3, %4 offset:3072"
                 :: "v"(v0), "v"(v1), "v"(v2), "v"(v3), "s"(sbase), "s"(ldsdst) : "memory");
}
template <int N> __device__ __forceinline__ void wait_bar() {
    SBAR();
    asm volatile("s_waitcnt vmcnt(%0)\n\ts_barrier" :: "n"(N) : "memory");
    SBAR();
}
__device__ __forceinline__ void bar() { SBAR(); asm volatile("s_barrier" ::: "memory"); SBAR(); }
__device__ __forceinline__ void bar0() { SBAR(); asm volatile("s_waitcnt lgkmcnt(0)\n\ts_barrier" ::: "memory"); SBAR(); }
__device__ __forceinline__ void bar_l1() {
    SBAR();
    asm volatile("s_waitcnt lgkmcnt(1)\n\ts_barrier" ::: "memory");
    SBAR();
}
__device__ __forceinline__ bf16x8 ld128(lds_cptr p) { return *(const LAS bf16x8*)p; }
__device__ __forceinline__ bf16x8 ldtr(lds_cptr p) {
    s16x4 lo = __builtin_amdgcn_ds_read_tr16_b64_v4i16((LAS s16x4*)p);
    s16x4 hi = __builtin_amdgcn_ds_read_tr16_b64_v4i16((LAS s16x4*)(p + 512));
    return (bf16x8){lo[0], lo[1], lo[2], lo[3], hi[0], hi[1], hi[2], hi[3]};
}
__device__ __forceinline__ u32x4 pack8(const f32x16& a, int s, float sc) {
    u32x4 w;
    w[0] = cvtpk(a[8 * s + 0] * sc, a[8 * s + 1] * sc); w[1] = cvtpk(a[8 * s + 2] * sc, a[8 * s + 3] * sc);
    w[2] = cvtpk(a[8 * s + 4] * sc, a[8 * s + 5] * sc); w[3] = cvtpk(a[8 * s + 6] * sc, a[8 * s + 7] * sc);
    return w;
}
#define STF " sc1"
#define ST16(p, v) do { const u32x4 v_ = (v); asm volatile("global_store_dwordx4 %0, %1, off" STF "\n\ts_nop 1" :: "v"(p), "v"(v_) : "memory"); } while (0)
__device__ __forceinline__ int swap23(int i) { return (i & ~12) | ((i & 4) << 1) | ((i & 8) >> 1); }

__device__ __forceinline__ int lf_order(int tau, int a, int c, int G) {
    if (tau == 0) return a;
    const int g = (tau - 1) >> 2, pos = (tau - 1) & 3;
    if (g != G - 1 && pos == c) return 4 * (g + 1) + a;
    const int b = (g == G - 1 || pos < c) ? pos : pos - 1;
    return 4 * g + (b < a ? b : b + 1);
}
#define NX3(v) ((v) == 2 ? 0 : (v) + 1)
#define NX5(v) ((v) == 4 ? 0 : (v) + 1)

__device__ __forceinline__ int rswz(int row) { return ((row >> 1) & 1) | (((row >> 3) & 3) << 1); }
__device__ __forceinline__ u32x4 pack44(const f32x4& a, const f32x4& b, float sc) {
    return (u32x4){cvtpk(a[0] * sc, a[1] * sc), cvtpk(a[2] * sc, a[3] * sc), cvtpk(b[0] * sc, b[1] * sc), cvtpk(b[2] * sc, b[3] * sc)};
}
__device__ __forceinline__ void drain() { asm volatile("s_waitcnt vmcnt(0)" ::: "memory"); }
typedef unsigned long long u64;
__device__ __forceinline__ u64 ld_tag(const char* p) { return __hip_atomic_load((const u64*)p, __ATOMIC_RELAXED, __HIP_MEMORY_SCOPE_AGENT); }
__device__ __forceinline__ void st_tag(char* p, u64 v) { __hip_atomic_store((u64*)p, v, __ATOMIC_RELAXED, __HIP_MEMORY_SCOPE_AGENT); }
__device__ __forceinline__ void tags_acquire(const char* p, u64 v, u64 tag) {
#pragma nounroll
    for (int spin = 0; !__all(v == tag) && spin < (1 << 22); ++spin) { __builtin_amdgcn_s_sleep(2); v = ld_tag(p); }
    asm volatile("" ::: "memory");
}
__device__ __forceinline__ u64 call_tag(const char* ws) {
    const u64 e = ld_tag(ws + WS_E) + 1;
    return ((u64)(unsigned)__builtin_amdgcn_readfirstlane((int)(e >> 32)) << 32) | (unsigned)__builtin_amdgcn_readfirstlane((int)e);
}
__device__ __forceinline__ int fresh_lane() {
    int l; asm volatile("v_mbcnt_lo_u32_b32 %0, -1, 0\n\tv_mbcnt_hi_u32_b32 %0, -1, %0" : "=v"(l)); return l;
}
__device__ __forceinline__ void bars(int n) { for (int i = 0; i < n; ++i) bar(); }

constexpr int M_KS = 8192, M_G0 = 4 * M_KS, M_GS = 32768, M_H = 16384, M_LDS = M_G0 + 4 * M_GS;
constexpr int NA = 32, NY = 44, NO = 76;
__global__ __launch_bounds__(1024, 4) void k_all(const u16* __restrict__ x, const u16* __restrict__ cr, const u16* __restrict__ ci,
                                                  const u16* __restrict__ hr, const u16* __restrict__ hi, const u16* __restrict__ wqkv,
                                                  const u16* __restrict__ wo, const u16* __restrict__ gw, char* ws, u16* __restrict__ out) {
    extern __shared__ __attribute__((aligned(16))) char smem[];
    LAS char* lds = (LAS char*)smem;
    DIAG_BEGIN();
    const int w = (__builtin_amdgcn_readfirstlane(threadIdx.x >> 6) + 12) & 15;
    const int xcd = blockIdx.x & 7, idx = blockIdx.x >> 3;
    const int rt = (xcd & 1) * 8 + (idx & 7), j = (xcd >> 1) * 4 + (idx >> 3);
    const int head = xcd * 4 + (idx >> 3), qb = idx & 7;
    const int part = xcd >> 2, tt = ((xcd >> 1) & 1) * 8 + (idx & 7), et = (xcd & 1) * 4 + (idx >> 3);
    const char* kb = ws + WS_KV + (size_t)head * 131072;
#define LANE_VARS() const int lane = fresh_lane(), r = lane & 31, hh = lane >> 5, c = lane & 15, g4 = lane >> 4, sg0 = 8 * (c >> 2) + (c & 3); \
    lds_cptr lp = (lds_cptr)lds + lane * 16; (void)r; (void)hh; (void)g4; (void)sg0; (void)lp
    char* myflag = ws + WS_F + blockIdx.x * 8;
    if (w >= 12) {
        LANE_VARS();
        const int g = w - 12;
        const int la = ((idx >> 3) - (idx & 7)) & 3, lc = (idx >> 1) & 3;
        const int tab16 = lf_order(lane & 15, la, lc, 4), tab32 = lf_order(lane & 31, la, lc, 8);
        const unsigned lds0 = (unsigned)(uintptr_t)lds;
        const unsigned vL = (unsigned)(lane * 16);
        unsigned vR[4], vW[2], vC[4], vA[2], vT[2];
#pragma unroll
        for (int i = 0; i < 4; ++i) {
            vR[i] = (unsigned)((8 * i + (lane >> 3)) * 2048 + (((lane & 7) ^ rswz(8 * i + (lane >> 3))) << 4) - i * 1024);
            vC[i] = (unsigned)((8 * i + (lane >> 3)) * 4096 + (((lane & 7) ^ ((4 * i + (lane >> 4)) & 7)) << 4) - i * 1024);
        }
#pragma unroll
        for (int i = 0; i < 2; ++i) {
            vW[i] = (unsigned)((8 * i + (lane >> 3)) * 6144 + (((lane & 7) ^ (((lane >> 4) & 1) << 2) ^ (i << 1)) << 4) - i * 1024);
            vT[i] = (unsigned)((8 * i + (lane >> 3)) * 2048 + (((lane & 7) ^ (((lane >> 4) & 1) << 2)) << 4) - i * 1024);
            vA[i] = (unsigned)(lane * 16 + i * (65536 - 2048));
        }
        const unsigned vKV = (unsigned)(lane * 16 + (4 << 20) - 3072 - 1024);
        const char* xb = (const char*)x + (size_t)(rt * 128 + g * 32) * 2048;
        const char* hb = (const char*)(j < 8 ? hr : hi) + (size_t)((j & 7) * 128 + g * 32) * 2048;
        const char* wb = (const char*)wqkv + (size_t)g * 16 * 6144 + j * 128;
        const char* cb = (const char*)(part ? ci : cr) + (size_t)(tt * 128 + 32 * g) * 4096;
        const char* ab = ws + WS_AB + (size_t)(part * 64 + et * 8 + 2 * g) * 65536;
        const char* ob = ws + WS_O + (size_t)(tt * 4 + g) * 65536;
        const char* wob = (const char*)wo + (2 * et + part) * 128;
        const unsigned dG = lds0 + M_G0 + g * 4096, dK = lds0 + g * 2048;
        auto sP = [&](int n) {
            const int kp = __builtin_amdgcn_readlane(tab16, n); const unsigned so = (n & 3) * M_GS;
            const char* b = wb + (size_t)(kp * (64 * 6144));
            glds4(vR[0], vR[1], vR[2], vR[3], xb + kp * 128, dG + so);
            glds2(vW[0], vW[1], b, dG + so + M_H - g * 2048); glds2(vW[0], vW[1], b + 2048, dG + so + M_H + 8192 - g * 2048);
            glds2(vW[0], vW[1], b + 4096, dK + (n & 3) * M_KS);
        };
        auto sH = [&](int n) {
            const int kp = __builtin_amdgcn_readlane(tab16, n & 15); const unsigned so = (n & 3) * M_GS;
            glds4(vR[0], vR[1], vR[2], vR[3], xb + kp * 128, dG + so); glds4(vR[0], vR[1], vR[2], vR[3], hb + kp * 128, dG + so + M_H);
        };
        auto sKV = [&](int n) {
            glds2d(vL, vKV, kb + ((n - NA) * 4096 + 4096 + g * 1024), dK + (n & 3) * M_KS);
        };
        auto sY = [&](int n) {
            const int kp = __builtin_amdgcn_readlane(tab32, n - NY); const unsigned so = (n & 3) * M_GS;
            glds4(vC[0], vC[1], vC[2], vC[3], cb + kp * 128, dG + so); glds4d(vA[0], vA[0], vA[1], vA[1], ab + kp * 2048, dG + so + M_H);
        };
        auto sO = [&](int n) {
            const int kp = __builtin_amdgcn_readlane(tab16, n - NO); const unsigned so = (n & 3) * M_GS;
            glds2(vT[0], vT[1], wob + (size_t)((kp * 64 + g * 16) * 2048), dG + so + M_H - g * 2048);
            glds4d(vL, vL, vL, vL, ob + kp * 4096, dG + so);
        };
#define STAMP()
#define STAMP_DUMP()
#define WB(N) do { wait_bar<N>(); STAMP(); } while (0)
        auto sC = [&](int n) { const int kp = __builtin_amdgcn_readlane(tab32, n - NY); glds4(vC[0], vC[1], vC[2], vC[3], cb + kp * 128, dG + (n & 3) * M_GS); };
        auto sAB = [&](int n) { const int kp = __builtin_amdgcn_readlane(tab32, n - NY); glds4d(vA[0], vA[0], vA[1], vA[1], ab + kp * 2048, dG + (n & 3) * M_GS + M_H); };
        sP(0); sP(1); WB(10);
        sP(2);
        for (int k = 1; k < 14; ++k) { sP(k + 2); WB(20); }
        sH(16); WB(18);
        for (int k = 15; k < 30; ++k) { sH(k + 2); WB(16); }
        glds16d(vL, kb + g * 1024, dK + 3 * M_KS); glds16d(vL, kb + (4 << 20), dK + 3 * M_KS + 1024);
        WB(10);
        for (int k = 31; k < 36; ++k) { sKV(k + 1); WB(2); }
        sKV(37); sC(44); WB(6);
        for (int k = 37; k < 40; ++k) { sKV(k + 1); sC(k + 8); WB(10); }
        sKV(41); WB(6);
        sKV(42); WB(2);
        sKV(43); WB(2);
        sKV(44); sAB(44); sAB(45); WB(10);
        sKV(45); sAB(46); WB(10);
        sKV(46); sAB(47); WB(10);
        sKV(47); sY(48); WB(14);
        for (int k = 47; k < 62; ++k) { sKV(k + 1); sY(k + 2); WB(18); }
        { const int vo = g < 3 ? 31 * 4096 + 1024 + g * 1024 : 0;
          glds16d(vL, kb + (4 << 20) + vo, dK + 3 * M_KS + 1024); sY(64); WB(17); }
        for (int k = 63; k < 70; ++k) { sY(k + 2); WB(16); }
        glds16d(vL, ws + WS_G + (size_t)tt * 1024, lds0 + 16384);
        sY(72); WB(17);
        sY(73); WB(17);
        sY(74); WB(16);
        sY(75); WB(16);
        WB(8);
        sO(76);
        for (int k = 75; k < 89; ++k) { sO(k + 2); WB(12); }
        sO(91); glds4d(vL, vL, vL, vL, ws + WS_Y + (size_t)(blockIdx.x ^ 4) * 16384 + g * 4096, lds0 + 16384 + g * 4096);
        WB(16);
        WB(10);
        WB(0);
        static_assert(NA == 32 && NY == 44 && NO == 76, "the DMA program is written for this schedule");
        STAMP_DUMP();
    } else if (w >= 8) {
        LANE_VARS();
        const int g = w - 8, gm = g >> 1, gn = g & 1;
        const u64 tag = call_tag(ws);
        __builtin_amdgcn_s_setprio(3);
        bars(2);
        {
            const u32x4* xr = (const u32x4*)(x + (size_t)(blockIdx.x * 8 + 2 * g) * 1024); const u32x4* gr = (const u32x4*)gw;
            const u32x4 a0 = xr[lane], a1 = xr[lane + 64], a2 = xr[lane + 128], a3 = xr[lane + 192], b0 = gr[lane], b1 = gr[lane + 64];
            bars(3);
            float z0 = 0.f, z1 = 0.f;
#pragma unroll
            for (int e = 0; e < 4; ++e) {
                z0 = fmaf(__uint_as_float(a0[e] << 16), __uint_as_float(b0[e] << 16), z0); z0 = fmaf(__uint_as_float(a0[e] & 0xffff0000u), __uint_as_float(b0[e] & 0xffff0000u), z0);
                z0 = fmaf(__uint_as_float(a1[e] << 16), __uint_as_float(b1[e] << 16), z0); z0 = fmaf(__uint_as_float(a1[e] & 0xffff0000u), __uint_as_float(b1[e] & 0xffff0000u), z0);
            }
            PIN(z0);
            bar();
#pragma unroll
            for (int e = 0; e < 4; ++e) {
                z1 = fmaf(__uint_as_float(a2[e] << 16), __uint_as_float(b0[e] << 16), z1); z1 = fmaf(__uint_as_float(a2[e] & 0xffff0000u), __uint_as_float(b0[e] & 0xffff0000u), z1);
                z1 = fmaf(__uint_as_float(a3[e] << 16), __uint_as_float(b1[e] << 16), z1); z1 = fmaf(__uint_as_float(a3[e] & 0xffff0000u), __uint_as_float(b1[e] & 0xffff0000u), z1);
            }
            PIN(z1);
#pragma unroll
            for (int o = 32; o >= 1; o >>= 1) { bar(); z0 += __shfl_xor(z0, o); z1 += __shfl_xor(z1, o); PIN(z0); PIN(z1); }
            bar();
            const float gt = bfr(1.f / (1.f + __expf(-bfr((lane & 1) ? z1 : z0))));
            if (lane < 2) st_tag(ws + WS_G + (size_t)(blockIdx.x * 8 + 2 * g + lane) * 8, ((u64)__float_as_uint(bfr(1.f - gt)) << 32) | __float_as_uint(gt));
            __builtin_amdgcn_s_setprio(0);
            bars(2);
        }
        f32x4 y[4][4];
        bf16x8 cA[4], cB[4], fa[4];
        lds_cptr ca[2][2], fp[2][2];
#define G_MM(SW, a_, CX, m_) y[a_][m_] = (SW) ? MFMA16(CX[m_], fa[a_], y[a_][m_]) : MFMA16(fa[a_], CX[m_], y[a_][m_])
#define G_STEP(SW, CP, CC, ST, HF, CO, FO) do { \
            _Pragma("unroll") for (int s_ = 0; s_ < 4; ++s_) { \
                _Pragma("unroll") for (int m = 0; m < 4; ++m) { \
                    if (s_ < 2) G_MM(SW, s_ + 2, CP, m); else G_MM(SW, s_ - 2, CC, m); \
                    if (s_ == 0) CC[m] = ld128(ca[(ST) >> 1][HF] + ((ST) & 1) * M_GS + CO(m)); \
                    if (m == 0 && s_ < 3) fa[s_] = ld128(fp[(ST) >> 1][HF] + ((ST) & 1) * M_GS + FO(s_)); \
                    if (m == 2 && s_ == 2) fa[3] = ld128(fp[(ST) >> 1][HF] + ((ST) & 1) * M_GS + FO(3)); \
                    SBAR(); } } } while (0)
#define G_ITER(SW, st, CO, FO) do { bar0(); G_STEP(SW, cB, cA, st, 0, CO, FO); G_STEP(SW, cA, cB, st, 1, CO, FO); } while (0)
#define G_RUN(SW, NIT, CO, FO, PRE) do { \
            _Pragma("unroll") for (int a = 0; a < 4; ++a) { cB[a] = (bf16x8){}; fa[a] = (bf16x8){}; \
                _Pragma("unroll") for (int b = 0; b < 4; ++b) y[a][b] = (f32x4){}; } \
            PRE; LGKM0(); \
            for (int it = 0; it < NIT; ++it) { G_ITER(SW, 0, CO, FO); G_ITER(SW, 1, CO, FO); G_ITER(SW, 2, CO, FO); G_ITER(SW, 3, CO, FO); } \
            bar0(); \
            _Pragma("unroll") for (int a = 2; a < 4; ++a) _Pragma("unroll") for (int m = 0; m < 4; ++m) G_MM(SW, a, cB, m); } while (0)
#define OFF_SIG(i) (((i) >> 1) * 4096 + ((i) & 1) * 512)
#define OFF_2K(i) ((i) * 2048)
#pragma unroll
        for (int h = 0; h < 2; ++h)
#pragma unroll
            for (int hf = 0; hf < 2; ++hf) {
                const int ra = M_G0 + h * 2 * M_GS + sg0 * 128 + (((4 * hf + g4) ^ rswz(sg0)) << 4);
                ca[h][hf] = (lds_cptr)lds + ra + gm * 8192; fp[h][hf] = (lds_cptr)lds + ra + M_H + gn * 8192; PIN(ca[h][hf]); PIN(fp[h][hf]);
            }
        G_RUN(true, 4, OFF_SIG, OFF_SIG, drain(); bar());
      { LANE_VARS();
#pragma unroll
        for (int gr = 0; gr < 2; ++gr) {
            char* abp = ws + WS_AB + ((size_t)(j * 8 + gn * 4) * 64 + rt * 4 + gm * 2 + gr) * 1024 + lane * 16;
#pragma unroll
            for (int b = 0; b < 4; ++b) ST16(abp + (size_t)b * 65536, pack44(y[b][2 * gr], y[b][2 * gr + 1], 1.f));
            bar();
        }
        bar();
        drain();
        bar();
        if (g == 0) {
            if (lane == 0) st_tag(myflag + 2048, tag);
            const int jj = part * 8 + et, rr = lane & 15;
            const char* fpn = ws + WS_F + 2048 + (((rr & 7) | ((jj & 3) << 3)) * 8 + ((rr >> 3) | ((jj >> 2) << 1))) * 8;
            bars(4);
            const u64 v = ld_tag(fpn);
            bar();
            tags_acquire(fpn, v, tag);
        } else bars(5);
        bar();
#pragma unroll
        for (int h = 0; h < 2; ++h)
#pragma unroll
            for (int hf = 0; hf < 2; ++hf) {
                ca[h][hf] = (lds_cptr)lds + M_G0 + h * 2 * M_GS + (gm * 64 + c) * 128 + (((4 * hf + g4) ^ ((c >> 1) & 7)) << 4);
                fp[h][hf] = lp + M_G0 + h * 2 * M_GS + M_H + gn * 8192 + hf * 1024; PIN(ca[h][hf]); PIN(fp[h][hf]);
            }
      }
        G_RUN(false, 8, OFF_2K, OFF_2K, bar());
      { LANE_VARS();
        __builtin_amdgcn_s_setprio(3);
#pragma unroll
        for (int m = 0; m < 4; ++m) {
            float gy = *(const LAS float*)(lds + 16384 + (gm * 64 + m * 16 + c) * 8);
            if (part) gy = -gy;
#pragma unroll
            for (int e = 0; e < 2; ++e) {
                const int off = ((gm * 2 + (m >> 1)) * 2 + e) * 2048 + (g4 >> 1) * 1024 + ((g4 & 1) * 32 + (m & 1) * 16 + c) * 16;
                const u32x4 v = pack44(y[2 * e][m], y[2 * e + 1][m], gy);
                if (gn == part) *(LAS u32x4*)(lds + off) = v; else ST16(ws + WS_Y + (size_t)blockIdx.x * 16384 + off, v);
            }
            bar();
        }
        bars(2);
        asm volatile("s_waitcnt vmcnt(0) lgkmcnt(0)" ::: "memory");
        bar();
        if (g == 0) {
            if (lane == 0) st_tag(myflag + 6144, tag);
            const char* fpn = ws + WS_F + 6144 + (blockIdx.x ^ 4) * 8;
            bars(3);
            const u64 v = ld_tag(fpn);
            bar();
            tags_acquire(fpn, v, tag);
            bar();
        }
      }
    } else {
        LANE_VARS();
        const u64 tag = call_tag(ws);
        {
            const int wm = w >> 1, wn = w & 1;
            lds_cptr xa[2][2], tq[2][2], tv[2];
            { const int q4 = c >> 2, p4 = c & 3;
#pragma unroll
              for (int i = 0; i < 2; ++i) {
                  const int tbo = (8 * g4 + q4) * 128 + ((((wn * 4 + i * 2) ^ ((q4 >> 1) << 2) ^ ((g4 & 1) << 1)) + (p4 >> 1)) << 4) + (p4 & 1) * 8;
                  tv[i] = (lds_cptr)lds + tbo; PIN(tv[i]);
#pragma unroll
                  for (int h = 0; h < 2; ++h) {
                      xa[h][i] = (lds_cptr)lds + M_G0 + h * 2 * M_GS + wm * 4096 + sg0 * 128 + (((4 * i + g4) ^ rswz(sg0)) << 4); PIN(xa[h][i]);
                      tq[h][i] = (lds_cptr)lds + M_G0 + h * 2 * M_GS + M_H + tbo; PIN(tq[h][i]);
                  } } }
            f32x4 acc[6][2];
            bf16x8 xA[2], xB[2], fb[6];
#pragma unroll
            for (int b = 0; b < 6; ++b) { acc[b][0] = (f32x4){}; acc[b][1] = (f32x4){}; }
            xB[0] = xB[1] = fb[3] = fb[4] = fb[5] = (bf16x8){};
#define Q_MM(b_, XF, m_) acc[b_][m_] = (b_) < 4 ? MFMA16(fb[b_], XF[m_], acc[b_][m_]) : MFMA16(XF[m_], fb[b_], acc[b_][m_])
#define Q_STEP(XP, XC, ST, HF) do { \
                _Pragma("unroll") for (int s_ = 0; s_ < 6; ++s_) { \
                    _Pragma("unroll") for (int m = 0; m < 2; ++m) { \
                        if (s_ < 3) Q_MM(s_ + 3, XP, m); else Q_MM(s_ - 3, XC, m); \
                        SBAR(); \
                        if (s_ == 0) XC[m] = ld128(xa[(ST) >> 1][HF] + ((ST) & 1) * M_GS + m * 512); \
                        { const int gb_ = s_ == 0 ? (m == 0 ? 0 : -1) : (s_ < 3 ? 2 * s_ - 1 + m : (s_ == 3 && m == 0 ? 5 : -1)); \
                          if (gb_ >= 0) fb[gb_ < 0 ? 0 : gb_] = ldtr((HF) * 4096 + (gb_ < 4 ? tq[(ST) >> 1][gb_ & 1] + ((ST) & 1) * M_GS + (gb_ >> 1) * 8192 \
                                                                                           : tv[gb_ & 1] + (ST) * M_KS)); } \
                        SBAR(); } } } while (0)
#define Q_ITER(st) do { bar0(); Q_STEP(xB, xA, st, 0); Q_STEP(xA, xB, st, 1); } while (0)
            LGKM0();
            for (int it = 0; it < 4; ++it) { Q_ITER(0); Q_ITER(1); Q_ITER(2); Q_ITER(3); }
            bar0();
#pragma unroll
            for (int b = 3; b < 6; ++b) { Q_MM(b, xB, 0); Q_MM(b, xB, 1); }
            DIAG_MARK(0);
            const int hd = 2 * j + wn, t32 = rt * 4 + wm;
            const float qs = 0.17677669529663687f * 1.4426950408889634f;
            const int fo = (g4 >> 1) * 1024 + (g4 & 1) * 512;
            char* qp = ws + WS_Q + (size_t)(hd * 64 + t32) * 2048 + fo;
            char* kp = ws + WS_KV + (size_t)(hd * 64 + t32) * 2048 + fo;
#pragma unroll
            for (int a = 0; a < 2; ++a) ST16(kp + (16 * (c >> 3) + 8 * a + (c & 7)) * 16, pack44(acc[2][a], acc[3][a], 1.f));
            bar();
#pragma unroll
            for (int a = 0; a < 2; ++a) ST16(kp + (4u << 20) + (16 * a + swap23(c)) * 16, pack44(acc[4 + a][0], acc[4 + a][1], 1.f));
            bar();
#pragma unroll
            for (int a = 0; a < 2; ++a) ST16(qp + (sg0 + 4 * a) * 16, pack44(acc[0][a], acc[1][a], qs));
        }
        bars(3);
        drain();
        bar();
        if (w == 0) {
            if (lane == 0) st_tag(myflag, tag);
            const int jj = head >> 1, rr = lane & 15;
            const int pb = lane < 16 ? ((rr & 7) | ((jj & 3) << 3)) * 8 + ((rr >> 3) | ((jj >> 2) << 1)) : (lane < 32 ? tt * 16 + rr : qb * 32 + (lane - 32));
            const char* fpn = ws + WS_F + pb * 8;
            bars(4);
            const u64 v = ld_tag(fpn);
            bars(2);
            tags_acquire(fpn, v, tag);
        } else bars(6);
        bar();
        const int qt = qb * 8 + w;
        bf16x8 qf[2]; float omg;
        { const char* qp = ws + WS_Q + (size_t)(head * 64 + qt) * 2048 + lane * 16;
          asm volatile("global_load_dwordx4 %0, %1, off sc1" : "=v"(qf[0]) : "v"(qp) : "memory");
          asm volatile("global_load_dwordx4 %0, %1, off offset:1024 sc1" : "=v"(qf[1]) : "v"(qp) : "memory");
          asm volatile("global_load_dword %0, %1, off offset:4 sc1" : "=v"(omg) : "v"(ws + WS_G + (size_t)(qt * 32 + r) * 8) : "memory"); }
        bf16x8 sel; { const short one = (((lane & 1) == ((lane >> 4) & 1))) ? (short)0x3f80 : (short)0;
#pragma unroll
            for (int i = 0; i < 8; ++i) sel[i] = one; }
        f32x16 o = (f32x16){}; f32x4 l4 = (f32x4){};
        f32x16 negm, sA, sB;
        bf16x8 kf[2], vf0, vf1;
        u32x4 p0, p1;
        bars(2);
        asm volatile("s_waitcnt vmcnt(0)" : "+v"(qf[0]), "+v"(qf[1]), "+v"(omg) :: "memory");
        DIAG_MARK(1);
        kf[0] = ld128(lp + 3 * M_KS); kf[1] = ld128(lp + 3 * M_KS + 2048);
        sA = MFMA32(kf[0], qf[0], (f32x16){}); sA = MFMA32(kf[1], qf[1], sA);
        { float m = sA[0];
#pragma unroll
          for (int i = 1; i < 16; ++i) m = fmaxf(m, sA[i]);
          m = fmaxf(m, __shfl_xor(m, 32));
#pragma unroll
          for (int i = 0; i < 16; ++i) { negm[i] = -m; sA[i] -= m; } }
        kf[0] = ld128(lp + 3 * M_KS + 4096); kf[1] = ld128(lp + 3 * M_KS + 6144);
        vf0 = ld128(lp + 3 * M_KS + 1024);
        vf1 = vf0; p1 = (u32x4){0u, 0u, 0u, 0u};
#define EN(S, i, n) do { _Pragma("unroll") for (int e_ = 0; e_ < n; ++e_) S[i + e_] = EX2(S[i + e_]); } while (0)
#define A_STEP(SC, SN, v1o, kn, v0n) do { \
            SN = MFMA32(kf[0], qf[0], negm); PIN(SN); SBAR(); \
            EN(SC, 0, 3); PIN(SC); SBAR(); \
            o = MFMA32(vf1, PB(p1), o); PIN(o); SBAR(); \
            EN(SC, 3, 3); PIN(SC); SBAR(); \
            SN = MFMA32(kf[1], qf[1], SN); PIN(SN); SBAR(); \
            vf1 = ld128(lp + (v1o)); kf[0] = ld128(lp + (kn)); kf[1] = ld128(lp + (kn) + 2048); SBAR(); \
            EN(SC, 6, 2); p0[0] = cvtpk(SC[0], SC[1]); p0[1] = cvtpk(SC[2], SC[3]); PIN(SC); PIN(p0); SBAR(); \
            l4 = MFMA16(sel, PB(p1), l4); PIN(l4); SBAR(); \
            p0[2] = cvtpk(SC[4], SC[5]); p0[3] = cvtpk(SC[6], SC[7]); PIN(p0); SBAR(); EN(SC, 8, 2); PIN(SC); SBAR(); \
            o = MFMA32(vf0, PB(p0), o); PIN(o); SBAR(); \
            vf0 = ld128(lp + (v0n)); SBAR(); \
            EN(SC, 10, 3); PIN(SC); SBAR(); \
            l4 = MFMA16(sel, PB(p0), l4); PIN(l4); SBAR(); \
            EN(SC, 13, 3); \
            _Pragma("unroll") for (int i = 0; i < 4; ++i) p1[i] = cvtpk(SC[8 + 2 * i], SC[9 + 2 * i]); \
            PIN(p1); SBAR(); \
        } while (0)
#define A_ITER(st) do { \
            constexpr int b0 = (st) * M_KS; \
            bar(); \
            A_STEP(sA, sB, b0 + 1024, b0, b0 + 3072); \
            A_STEP(sB, sA, b0 + 5120, b0 + 4096, b0 + 7168); \
            } while (0)
        static_assert(NA % 4 == 0 && NY % 4 == 0 && NO % 4 == 0, "slot phase");
        LGKM0();
        for (int it = 0; it < 8; ++it) { A_ITER(0); A_ITER(1); A_ITER(2); A_ITER(3); }
        bar();
        o = MFMA32(vf1, PB(p1), o); l4 = MFMA16(sel, PB(p1), l4);
        DIAG_MARK(2);
        float l = ((lane >> 4) & 1) ? l4[1] : l4[0];
        bool ok = l < 1.0e30f;
#pragma unroll
        for (int i = 0; i < 16; ++i) ok = ok && (fabsf(o[i]) < 3.0e38f);
        if (!__all(ok)) {
            float m = -INFINITY, ls = 0.f; o = (f32x16){};
            const char* gp = kb + lane * 16;
            for (int t = 0; t < 64; ++t) {
                bf16x8 k0, k1, v0, v1;
                asm volatile("global_load_dwordx4 %0, %4, off sc1\n\tglobal_load_dwordx4 %1, %4, off offset:1024 sc1\n\t"
                             "global_load_dwordx4 %2, %5, off sc1\n\tglobal_load_dwordx4 %3, %5, off offset:1024 sc1\n\ts_waitcnt vmcnt(0)"
                             : "=&v"(k0), "=&v"(k1), "=&v"(v0), "=&v"(v1) : "v"(gp + t * 2048), "v"(gp + (4 << 20) + t * 2048) : "memory");
                f32x16 s = MFMA32(k0, qf[0], (f32x16){}); s = MFMA32(k1, qf[1], s);
                float tm = s[0];
#pragma unroll
                for (int i = 1; i < 16; ++i) tm = fmaxf(tm, s[i]);
                tm = fmaxf(tm, __shfl_xor(tm, 32));
                const float mn = fmaxf(m, tm), al = EX2(m - mn);
                m = mn; ls *= al;
#pragma unroll
                for (int i = 0; i < 16; ++i) { o[i] *= al; s[i] = EX2(s[i] - mn); ls += s[i]; }
                u32x4 a0, a1;
#pragma unroll
                for (int i = 0; i < 4; ++i) { a0[i] = cvtpk(s[2 * i], s[2 * i + 1]); a1[i] = cvtpk(s[8 + 2 * i], s[9 + 2 * i]); }
                o = MFMA32(v0, PB(a0), o); o = MFMA32(v1, PB(a1), o);
            }
            l = ls + __shfl_xor(ls, 32);
        }
        { const float sc = omg / l;
          char* op = ws + WS_O + ((size_t)qt * 64 + 2 * head) * 1024 + lane * 16;
#pragma unroll
          for (int s = 0; s < 2; ++s) ST16(op + s * 1024, pack8(o, s, sc)); }
        bars(2);
        drain();
        bar();
        if (w == 0) {
            if (lane == 0) st_tag(myflag + 4096, tag);
            const int hd = lane & 31;
            const char* fpn = ws + WS_F + 4096 + (((hd & 3) * 8 + (tt >> 1)) * 8 + (hd >> 2)) * 8;
            bars(4);
            const u64 v = ld_tag(fpn);
            bars(2);
            tags_acquire(fpn, v, tag);
        } else bars(6);
        bars(2);
        DIAG_MARK(3);
        {
            const int wm = w >> 1, wn = w & 1;
            const int q4 = (lane & 15) >> 2, p4 = lane & 3, cg = (lane >> 4) & 1, pi4 = ((p4 & 1) << 1) | (p4 >> 1);
            lds_cptr ap[2], bp[2];
#pragma unroll
            for (int h = 0; h < 2; ++h) {
                ap[h] = lp + M_G0 + h * 2 * M_GS + wm * 4096;
                bp[h] = (lds_cptr)lds + M_G0 + h * 2 * M_GS + M_H + (8 * hh + q4) * 128 + ((wn ^ (q4 >> 1)) << 6) + cg * 32 + pi4 * 8;
            }
            f32x16 acc = (f32x16){};
            bf16x8 a0[4], b0[4], a1[4], b1[4];
#pragma unroll
            for (int i = 0; i < 4; ++i) { a1[i] = (bf16x8){}; b1[i] = (bf16x8){}; }
#define F_ITER(st, AP, BP, AC, BC) do { \
                bar0(); \
                _Pragma("unroll") for (int i = 0; i < 4; ++i) { \
                    AC[i] = ld128(ap[(st) >> 1] + ((st) & 1) * M_GS + i * 1024); BC[i] = ldtr(bp[(st) >> 1] + ((st) & 1) * M_GS + i * 2048); SBAR(); \
                    acc = MFMA32(BP[i], AP[i], acc); PIN(acc); SBAR(); } \
                } while (0)
            LGKM0();
            for (int it = 0; it < 4; ++it) { F_ITER(0, a1, b1, a0, b0); F_ITER(1, a0, b0, a1, b1); F_ITER(2, a1, b1, a0, b0); F_ITER(3, a0, b0, a1, b1); }
#pragma unroll
            for (int i = 0; i < 4; ++i) acc = MFMA32(b1[i], a1[i], acc);
            DIAG_MARK(4);
#pragma unroll
            for (int s = 0; s < 2; ++s) {
                const u32x4 ya = *(const LAS u32x4*)(lp + w * 2048 + s * 1024), yb = *(const LAS u32x4*)(lp + 16384 + w * 2048 + s * 1024);
#pragma unroll
                for (int i = 0; i < 4; ++i) {
                    acc[8 * s + 2 * i] += __uint_as_float(ya[i] << 16) + __uint_as_float(yb[i] << 16);
                    acc[8 * s + 2 * i + 1] += __uint_as_float(ya[i] & 0xffff0000u) + __uint_as_float(yb[i] & 0xffff0000u);
                }
            }
            u16* op = out + (size_t)(tt * 128 + wm * 32 + r) * 1024 + (2 * et + part) * 64 + wn * 32 + hh * 8;
#pragma unroll
            for (int s = 0; s < 2; ++s) ST16(op + s * 16, pack8(acc, s, 1.f));
        }
        if (blockIdx.x == 0 && w == 0 && lane == 0) st_tag(ws + WS_E, tag);
        DIAG_END(0, ws);
    }
}

extern "C" void kernel_launch(void* const* d_in, const int* in_sizes, int n_in, void* d_out, int out_size, void* d_ws, size_t ws_size,
                              hipStream_t stream) {
    (void)hipFuncSetAttribute((const void*)k_all, hipFuncAttributeMaxDynamicSharedMemorySize, M_LDS);
    k_all<<<256, 1024, M_LDS, stream>>>((const u16*)d_in[0], (const u16*)d_in[1], (const u16*)d_in[2], (const u16*)d_in[3], (const u16*)d_in[4],
                                        (const u16*)d_in[6], (const u16*)d_in[7], (const u16*)d_in[8], (char*)d_ws, (u16*)d_out);
}
```

```cpp
#include <hip/hip_runtime.h>
#include <hip/hip_bf16.h>

#define WARM 0
#define RTSTAMP 0
typedef unsigned short u16;
typedef short bf16x8 __attribute__((ext_vector_type(8)));
typedef short s16x4 __attribute__((ext_vector_type(4)));
typedef float f32x16 __attribute__((ext_vector_type(16)));
typedef float f32x4 __attribute__((ext_vector_type(4)));
typedef float f32x2 __attribute__((ext_vector_type(2)));
typedef unsigned u32x4 __attribute__((ext_vector_type(4)));
#define LAS __attribute__((address_space(3)))
typedef LAS const char* lds_cptr;

constexpr size_t WS_Q = 0;
constexpr size_t WS_KV = 4u << 20;
constexpr size_t WS_AB = 12u << 20;
constexpr size_t WS_O = 20u << 20;
constexpr size_t WS_Y = 24u << 20;
constexpr size_t WS_G = 40u << 20;
constexpr size_t WS_F = WS_G + 65536;
constexpr size_t WS_E = WS_F + 16384;

#define DIAG_BEGIN()
#define DIAG_MARK(i)
#define DIAG_END(kid, wsp)

#define MFMA32(a, b, c) __builtin_amdgcn_mfma_f32_32x32x16_bf16((a), (b), (c), 0, 0, 0)
#define MFMA16(a, b, c) __builtin_amdgcn_mfma_f32_16x16x32_bf16((a), (b), (c), 0, 0, 0)
#define SBAR() __builtin_amdgcn_sched_barrier(0)
#define PIN(x) asm volatile("" : "+v"(x))
#define EX2(v) __builtin_amdgcn_exp2f(v)
#define PB(p) __builtin_bit_cast(bf16x8, p)
#define LGKM0() __builtin_amdgcn_s_waitcnt(0xC07F)

__device__ __forceinline__ unsigned cvtpk(float lo, float hi) {
    typedef __bf16 bf16x2_t __attribute__((ext_vector_type(2)));
    return __builtin_bit_cast(unsigned, __builtin_convertvector((f32x2){lo, hi}, bf16x2_t)); }
__device__ __forceinline__ float bfr(float f) { return __uint_as_float(cvtpk(f, 0.f) << 16); }

__device__ __forceinline__ void glds16(unsigned voff, const void* sbase, unsigned ldsdst) {
    asm volatile("s_mov_b32 m0, %2\n\ts_nop 0\n\tglobal_load_lds_dwordx4 %0, %1" :: "v"(voff), "s"(sbase), "s"(ldsdst) : "memory");
}
#define DSC " sc1"
__device__ __forceinline__ void glds16d(unsigned voff, const void* sbase, unsigned ldsdst) {
    asm volatile("s_mov_b32 m0, %2\n\ts_nop 0\n\tglobal_load_lds_dwordx4 %0, %1" DSC :: "v"(voff), "s"(sbase), "s"(ldsdst) : "memory");
}
__device__ __forceinline__ void glds2d(unsigned v0, unsigned v1, const void* sbase, unsigned ldsdst) {
    asm volatile("s_mov_b32 m0, %3\n\ts_nop 0\n\tglobal_load_lds_dwordx4 %0, %2" DSC "\n\tglobal_load_lds_dwordx4 %1, %2 offset:1024" DSC
                 :: "v"(v0), "v"(v1), "s"(sbase), "s"(ldsdst) : "memory");
}
__device__ __forceinline__ void glds4d(unsigned v0, unsigned v1, unsigned v2, unsigned v3, const void* sbase, unsigned ldsdst) {
    asm volatile("s_mov_b32 m0, %5\n\ts_nop 0\n\tglobal_load_lds_dwordx4 %0, %4" DSC "\n\tglobal_load_lds_dwordx4 %1, %4 offset:1024" DSC "\n\t"
                 "global_load_lds_dwordx4 %2, %4 offset:2048" DSC "\n\tglobal_load_lds_dwordx4 %3, %4 offset:3072" DSC
                 :: "v"(v0), "v"(v1), "v"(v2), "v"(v3), "s"(sbase), "s"(ldsdst) : "memory");
}
__device__ __forceinline__ void glds2(unsigned v0, unsigned v1, const void* sbase, unsigned ldsdst) {
    asm volatile("s_mov_b32 m0, %3\n\ts_nop 0\n\tglobal_load_lds_dwordx4 %0, %2\n\tglobal_load_lds_dwordx4 %1, %2 offset:1024"
                 :: "v"(v0), "v"(v1), "s"(sbase), "s"(ldsdst) : "memory");
}
__device__ __forceinline__ void glds4(unsigned v0, unsigned v1, unsigned v2, unsigned v3, const void* sbase, unsigned ldsdst) {
    asm volatile("s_mov_b32 m0, %5\n\ts_nop 0\n\tglobal_load_lds_dwordx4 %0, %4\n\tglobal_load_lds_dwordx4 %1, %4 offset:1024\n\t"
                 "global_load_lds_dwordx4 %2, %4 offset:2048\n\tglobal_load_lds_dwordx4 %3, %4 offset:3072"
                 :: "v"(v0), "v"(v1), "v"(v2), "v"(v3), "s"(sbase), "s"(ldsdst) : "memory");
}
template <int N> __device__ __forceinline__ void wait_bar() {
    SBAR();
    asm volatile("s_waitcnt vmcnt(%0)\n\ts_barrier" :: "n"(N) : "memory");
    SBAR();
}
__device__ __forceinline__ void bar() { SBAR(); asm volatile("s_barrier" ::: "memory"); SBAR(); }
__device__ __forceinline__ void bar0() { SBAR(); asm volatile("s_waitcnt lgkmcnt(0)\n\ts_barrier" ::: "memory"); SBAR(); }
__device__ __forceinline__ void bar_l1() {
    SBAR();
    asm volatile("s_waitcnt lgkmcnt(1)\n\ts_barrier" ::: "memory");
    SBAR();
}
__device__ __forceinline__ bf16x8 ld128(lds_cptr p) { return *(const LAS bf16x8*)p; }
__device__ __forceinline__ bf16x8 ldtr(lds_cptr p) {
    s16x4 lo = __builtin_amdgcn_ds_read_tr16_b64_v4i16((LAS s16x4*)p);
    s16x4 hi = __builtin_amdgcn_ds_read_tr16_b64_v4i16((LAS s16x4*)(p + 512));
    return (bf16x8){lo[0], lo[1], lo[2], lo[3], hi[0], hi[1], hi[2], hi[3]};
}
__device__ __forceinline__ u32x4 pack8(const f32x16& a, int s, float sc) {
    u32x4 w;
    w[0] = cvtpk(a[8 * s + 0] * sc, a[8 * s + 1] * sc); w[1] = cvtpk(a[8 * s + 2] * sc, a[8 * s + 3] * sc);
    w[2] = cvtpk(a[8 * s + 4] * sc, a[8 * s + 5] * sc); w[3] = cvtpk(a[8 * s + 6] * sc, a[8 * s + 7] * sc);
    return w;
}
#define STF " sc1"
#define ST16(p, v) do { const u32x4 v_ = (v); asm volatile("global_store_dwordx4 %0, %1, off" STF "\n\ts_nop 1" :: "v"(p), "v"(v_) : "memory"); } while (0)
__device__ __forceinline__ int swap23(int i) { return (i & ~12) | ((i & 4) << 1) | ((i & 8) >> 1); }

__device__ __forceinline__ int lf_order(int tau, int a, int c, int G) {
    if (tau == 0) return a;
    const int g = (tau - 1) >> 2, pos = (tau - 1) & 3;
    if (g != G - 1 && pos == c) return 4 * (g + 1) + a;
    const int b = (g == G - 1 || pos < c) ? pos : pos - 1;
    return 4 * g + (b < a ? b : b + 1);
}
#define NX3(v) ((v) == 2 ? 0 : (v) + 1)
#define NX5(v) ((v) == 4 ? 0 : (v) + 1)

__device__ __forceinline__ int rswz(int row) { return ((row >> 1) & 1) | (((row >> 3) & 3) << 1); }
__device__ __forceinline__ u32x4 pack44(const f32x4& a, const f32x4& b, float sc) {
    return (u32x4){cvtpk(a[0] * sc, a[1] * sc), cvtpk(a[2] * sc, a[3] * sc), cvtpk(b[0] * sc, b[1] * sc), cvtpk(b[2] * sc, b[3] * sc)};
}
__device__ __forceinline__ void drain() { asm volatile("s_waitcnt vmcnt(0)" ::: "memory"); }
typedef unsigned long long u64;
__device__ __forceinline__ u64 ld_tag(const char* p) { return __hip_atomic_load((const u64*)p, __ATOMIC_RELAXED, __HIP_MEMORY_SCOPE_AGENT); }
__device__ __forceinline__ void st_tag(char* p, u64 v) { __hip_atomic_store((u64*)p, v, __ATOMIC_RELAXED, __HIP_MEMORY_SCOPE_AGENT); }
__device__ __forceinline__ void tags_acquire(const char* p, u64 v, u64 tag) {
#pragma nounroll
    for (int spin = 0; !__all(v == tag) && spin < (1 << 22); ++spin) { __builtin_amdgcn_s_sleep(2); v = ld_tag(p); }
    asm volatile("" ::: "memory");
}
__device__ __forceinline__ u64 call_tag(const char* ws) {
    const u64 e = ld_tag(ws + WS_E) + 1;
    return ((u64)(unsigned)__builtin_amdgcn_readfirstlane((int)(e >> 32)) << 32) | (unsigned)__builtin_amdgcn_readfirstlane((int)e);
}
__device__ __forceinline__ int fresh_lane() {
    int l; asm volatile("v_mbcnt_lo_u32_b32 %0, -1, 0\n\tv_mbcnt_hi_u32_b32 %0, -1, %0" : "=v"(l)); return l;
}
__device__ __forceinline__ void bars(int n) { for (int i = 0; i < n; ++i) bar(); }

constexpr int M_KS = 8192, M_G0 = 4 * M_KS, M_GS = 32768, M_H = 16384, M_LDS = M_G0 + 4 * M_GS;
constexpr int CODE_KB = 24;
constexpr int NA = 32, NY = 44, NO = 76;
__global__ __launch_bounds__(1024, 4) void k_all(const u16* __restrict__ x, const u16* __restrict__ cr, const u16* __restrict__ ci,
                                                  const u16* __restrict__ hr, const u16* __restrict__ hi, const u16* __restrict__ wqkv,
                                                  const u16* __restrict__ wo, const u16* __restrict__ gw, char* ws, u16* __restrict__ out) {
    extern __shared__ __attribute__((aligned(16))) char smem[];
    LAS char* lds = (LAS char*)smem;
    unsigned long long pc0; asm volatile("s_getpc_b64 %0" : "=s"(pc0));
    DIAG_BEGIN();
    const int w = (__builtin_amdgcn_readfirstlane(threadIdx.x >> 6) + 12) & 15;
    const int xcd = blockIdx.x & 7, idx = blockIdx.x >> 3;
    const int rt = (xcd & 1) * 8 + (idx & 7), j = (xcd >> 1) * 4 + (idx >> 3);
    const int head = xcd * 4 + (idx >> 3), qb = idx & 7;
    const int part = xcd >> 2, tt = ((xcd >> 1) & 1) * 8 + (idx & 7), et = (xcd & 1) * 4 + (idx >> 3);
    const char* kb = ws + WS_KV + (size_t)head * 131072;
#define LANE_VARS() const int lane = fresh_lane(), r = lane & 31, hh = lane >> 5, c = lane & 15, g4 = lane >> 4, sg0 = 8 * (c >> 2) + (c & 3); \
    lds_cptr lp = (lds_cptr)lds + lane * 16; (void)r; (void)hh; (void)g4; (void)sg0; (void)lp
    char* myflag = ws + WS_F + blockIdx.x * 8;
    if (w >= 12) {
        LANE_VARS();
        const int g = w - 12;
        const int la = ((idx >> 3) - (idx & 7)) & 3, lc = (idx >> 1) & 3;
        const int tab16 = lf_order(lane & 15, la, lc, 4), tab32 = lf_order(lane & 31, la, lc, 8);
        const unsigned lds0 = (unsigned)(uintptr_t)lds;
        const unsigned vL = (unsigned)(lane * 16);
        unsigned vR[4], vW[2], vC[4], vA[2], vT[2];
#pragma unroll
        for (int i = 0; i < 4; ++i) {
            vR[i] = (unsigned)((8 * i + (lane >> 3)) * 2048 + (((lane & 7) ^ rswz(8 * i + (lane >> 3))) << 4) - i * 1024);
            vC[i] = (unsigned)((8 * i + (lane >> 3)) * 4096 + (((lane & 7) ^ ((4 * i + (lane >> 4)) & 7)) << 4) - i * 1024);
        }
#pragma unroll
        for (int i = 0; i < 2; ++i) {
            vW[i] = (unsigned)((8 * i + (lane >> 3)) * 6144 + (((lane & 7) ^ (((lane >> 4) & 1) << 2) ^ (i << 1)) << 4) - i * 1024);
            vT[i] = (unsigned)((8 * i + (lane >> 3)) * 2048 + (((lane & 7) ^ (((lane >> 4) & 1) << 2)) << 4) - i * 1024);
            vA[i] = (unsigned)(lane * 16 + i * (65536 - 2048));
        }
        const unsigned vKV = (unsigned)(lane * 16 + (4 << 20) - 3072 - 1024);
        const char* xb = (const char*)x + (size_t)(rt * 128 + g * 32) * 2048;
        const char* hb = (const char*)(j < 8 ? hr : hi) + (size_t)((j & 7) * 128 + g * 32) * 2048;
        const char* wb = (const char*)wqkv + (size_t)g * 16 * 6144 + j * 128;
        const char* cb = (const char*)(part ? ci : cr) + (size_t)(tt * 128 + 32 * g) * 4096;
        const char* ab = ws + WS_AB + (size_t)(part * 64 + et * 8 + 2 * g) * 65536;
        const char* ob = ws + WS_O + (size_t)(tt * 4 + g) * 65536;
        const char* wob = (const char*)wo + (2 * et + part) * 128;
        const unsigned dG = lds0 + M_G0 + g * 4096, dK = lds0 + g * 2048;
        auto sP = [&](int n) {
            const int kp = __builtin_amdgcn_readlane(tab16, n); const unsigned so = (n & 3) * M_GS;
            const char* b = wb + (size_t)(kp * (64 * 6144));
            glds4(vR[0], vR[1], vR[2], vR[3], xb + kp * 128, dG + so);
            glds2(vW[0], vW[1], b, dG + so + M_H - g * 2048); glds2(vW[0], vW[1], b + 2048, dG + so + M_H + 8192 - g * 2048);
            glds2(vW[0], vW[1], b + 4096, dK + (n & 3) * M_KS);
        };
        auto sH = [&](int n) {
            const int kp = __builtin_amdgcn_readlane(tab16, n & 15); const unsigned so = (n & 3) * M_GS;
            glds4(vR[0], vR[1], vR[2], vR[3], xb + kp * 128, dG + so); glds4(vR[0], vR[1], vR[2], vR[3], hb + kp * 128, dG + so + M_H);
        };
        auto sKV = [&](int n) {
            glds2d(vL, vKV, kb + ((n - NA) * 4096 + 4096 + g * 1024), dK + (n & 3) * M_KS);
        };
        auto sY = [&](int n) {
            const int kp = __builtin_amdgcn_readlane(tab32, n - NY); const unsigned so = (n & 3) * M_GS;
            glds4(vC[0], vC[1], vC[2], vC[3], cb + kp * 128, dG + so); glds4d(vA[0], vA[0], vA[1], vA[1], ab + kp * 2048, dG + so + M_H);
        };
        auto sO = [&](int n) {
            const int kp = __builtin_amdgcn_readlane(tab16, n - NO); const unsigned so = (n & 3) * M_GS;
            glds2(vT[0], vT[1], wob + (size_t)((kp * 64 + g * 16) * 2048), dG + so + M_H - g * 2048);
            glds4d(vL, vL, vL, vL, ob + kp * 4096, dG + so);
        };
#define STAMP()
#define STAMP_DUMP()
#define WB(N) do { wait_bar<N>(); STAMP(); } while (0)
        auto sC = [&](int n) { const int kp = __builtin_amdgcn_readlane(tab32, n - NY); glds4(vC[0], vC[1], vC[2], vC[3], cb + kp * 128, dG + (n & 3) * M_GS); };
        auto sAB = [&](int n) { const int kp = __builtin_amdgcn_readlane(tab32, n - NY); glds4d(vA[0], vA[0], vA[1], vA[1], ab + kp * 2048, dG + (n & 3) * M_GS + M_H); };
        sP(0); sP(1); WB(10);
        sP(2);
        for (int k = 1; k < 14; ++k) { sP(k + 2); WB(20); }
        sH(16); WB(18);
        for (int k = 15; k < 30; ++k) { sH(k + 2); WB(16); }
        glds16d(vL, kb + g * 1024, dK + 3 * M_KS); glds16d(vL, kb + (4 << 20), dK + 3 * M_KS + 1024);
        WB(10);
        for (int k = 31; k < 36; ++k) { sKV(k + 1); WB(2); }
        sKV(37); sC(44); WB(6);
        for (int k = 37; k < 40; ++k) { sKV(k + 1); sC(k + 8); WB(10); }
        sKV(41); WB(6);
        sKV(42); WB(2);
        sKV(43); WB(2);
        sKV(44); sAB(44); sAB(45); WB(10);
        sKV(45); sAB(46); WB(10);
        sKV(46); sAB(47); WB(10);
        sKV(47); sY(48); WB(14);
        for (int k = 47; k < 62; ++k) { sKV(k + 1); sY(k + 2); WB(18); }
        { const int vo = g < 3 ? 31 * 4096 + 1024 + g * 1024 : 0;
          glds16d(vL, kb + (4 << 20) + vo, dK + 3 * M_KS + 1024); sY(64); WB(17); }
        for (int k = 63; k < 70; ++k) { sY(k + 2); WB(16); }
        glds16d(vL, ws + WS_G + (size_t)tt * 1024, lds0 + 16384);
        sY(72); WB(17);
        sY(73); WB(17);
        sY(74); WB(16);
        sY(75); WB(16);
        WB(8);
        sO(76);
        for (int k = 75; k < 89; ++k) { sO(k + 2); WB(12); }
        sO(91); glds4d(vL, vL, vL, vL, ws + WS_Y + (size_t)(blockIdx.x ^ 4) * 16384 + g * 4096, lds0 + 16384 + g * 4096);
        WB(16);
        WB(10);
        WB(0);
        static_assert(NA == 32 && NY == 44 && NO == 76, "the DMA program is written for this schedule");
        STAMP_DUMP();
    } else if (w >= 8) {
        LANE_VARS();
        const int g = w - 8, gm = g >> 1, gn = g & 1;
        { const int piece = (idx & 7) * 4 + g;
          if (piece < CODE_KB) { u32x4 v; asm volatile("global_load_dwordx4 %0, %1, %2\n\ts_waitcnt vmcnt(0)" : "=&v"(v) : "v"(piece * 1024 + lane * 16), "s"(pc0 & ~255ull) : "memory"); } }
        const u64 tag = call_tag(ws);
        __builtin_amdgcn_s_setprio(3);
        bars(2);
        {
            const u32x4* xr = (const u32x4*)(x + (size_t)(blockIdx.x * 8 + 2 * g) * 1024); const u32x4* gr = (const u32x4*)gw;
            const u32x4 a0 = xr[lane], a1 = xr[lane + 64], a2 = xr[lane + 128], a3 = xr[lane + 192], b0 = gr[lane], b1 = gr[lane + 64];
            bars(3);
            float z0 = 0.f, z1 = 0.f;
#pragma unroll
            for (int e = 0; e < 4; ++e) {
                z0 = fmaf(__uint_as_float(a0[e] << 16), __uint_as_float(b0[e] << 16), z0); z0 = fmaf(__uint_as_float(a0[e] & 0xffff0000u), __uint_as_float(b0[e] & 0xffff0000u), z0);
                z0 = fmaf(__uint_as_float(a1[e] << 16), __uint_as_float(b1[e] << 16), z0); z0 = fmaf(__uint_as_float(a1[e] & 0xffff0000u), __uint_as_float(b1[e] & 0xffff0000u), z0);
            }
            PIN(z0);
            bar();
#pragma unroll
            for (int e = 0; e < 4; ++e) {
                z1 = fmaf(__uint_as_float(a2[e] << 16), __uint_as_float(b0[e] << 16), z1); z1 = fmaf(__uint_as_float(a2[e] & 0xffff0000u), __uint_as_float(b0[e] & 0xffff0000u), z1);
                z1 = fmaf(__uint_as_float(a3[e] << 16), __uint_as_float(b1[e] << 16), z1); z1 = fmaf(__uint_as_float(a3[e] & 0xffff0000u), __uint_as_float(b1[e] & 0xffff0000u), z1);
            }
            PIN(z1);
#pragma unroll
            for (int o = 32; o >= 1; o >>= 1) { bar(); z0 += __shfl_xor(z0, o); z1 += __shfl_xor(z1, o); PIN(z0); PIN(z1); }
            bar();
            const float gt = bfr(1.f / (1.f + __expf(-bfr((lane & 1) ? z1 : z0))));
            if (lane < 2) st_tag(ws + WS_G + (size_t)(blockIdx.x * 8 + 2 * g + lane) * 8, ((u64)__float_as_uint(bfr(1.f - gt)) << 32) | __float_as_uint(gt));
            __builtin_amdgcn_s_setprio(0);
            bars(2);
        }
        f32x4 y[4][4];
        bf16x8 cA[4], cB[4], fa[4];
        lds_cptr ca[2][2], fp[2][2];
#define G_MM(SW, a_, CX, m_) y[a_][m_] = (SW) ? MFMA16(CX[m_], fa[a_], y[a_][m_]) : MFMA16(fa[a_], CX[m_], y[a_][m_])
#define G_STEP(SW, CP, CC, ST, HF, CO, FO) do { \
            _Pragma("unroll") for (int s_ = 0; s_ < 4; ++s_) { \
                _Pragma("unroll") for (int m = 0; m < 4; ++m) { \
                    if (s_ < 2) G_MM(SW, s_ + 2, CP, m); else G_MM(SW, s_ - 2, CC, m); \
                    if (s_ == 0) CC[m] = ld128(ca[(ST) >> 1][HF] + ((ST) & 1) * M_GS + CO(m)); \
                    if (m == 0 && s_ < 3) fa[s_] = ld128(fp[(ST) >> 1][HF] + ((ST) & 1) * M_GS + FO(s_)); \
                    if (m == 2 && s_ == 2) fa[3] = ld128(fp[(ST) >> 1][HF] + ((ST) & 1) * M_GS + FO(3)); \
                    SBAR(); } } } while (0)
#define G_ITER(SW, st, CO, FO) do { bar0(); G_STEP(SW, cB, cA, st, 0, CO, FO); G_STEP(SW, cA, cB, st, 1, CO, FO); } while (0)
#define G_RUN(SW, NIT, CO, FO, PRE) do { \
            _Pragma("unroll") for (int a = 0; a < 4; ++a) { cB[a] = (bf16x8){}; fa[a] = (bf16x8){}; \
                _Pragma("unroll") for (int b = 0; b < 4; ++b) y[a][b] = (f32x4){}; } \
            PRE; LGKM0(); \
            for (int it = 0; it < NIT; ++it) { G_ITER(SW, 0, CO, FO); G_ITER(SW, 1, CO, FO); G_ITER(SW, 2, CO, FO); G_ITER(SW, 3, CO, FO); } \
            bar0(); \
            _Pragma("unroll") for (int a = 2; a < 4; ++a) _Pragma("unroll") for (int m = 0; m < 4; ++m) G_MM(SW, a, cB, m); } while (0)
#define OFF_SIG(i) (((i) >> 1) * 4096 + ((i) & 1) * 512)
#define OFF_2K(i) ((i) * 2048)
#pragma unroll
        for (int h = 0; h < 2; ++h)
#pragma unroll
            for (int hf = 0; hf < 2; ++hf) {
                const int ra = M_G0 + h * 2 * M_GS + sg0 * 128 + (((4 * hf + g4) ^ rswz(sg0)) << 4);
                ca[h][hf] = (lds_cptr)lds + ra + gm * 8192; fp[h][hf] = (lds_cptr)lds + ra + M_H + gn * 8192; PIN(ca[h][hf]); PIN(fp[h][hf]);
            }
        G_RUN(true, 4, OFF_SIG, OFF_SIG, drain(); bar());
      { LANE_VARS();
#pragma unroll
        for (int gr = 0; gr < 2; ++gr) {
            char* abp = ws + WS_AB + ((size_t)(j * 8 + gn * 4) * 64 + rt * 4 + gm * 2 + gr) * 1024 + lane * 16;
#pragma unroll
            for (int b = 0; b < 4; ++b) ST16(abp + (size_t)b * 65536, pack44(y[b][2 * gr], y[b][2 * gr + 1], 1.f));
            bar();
        }
        bar();
        drain();
        bar();
        if (g == 0) {
            if (lane == 0) st_tag(myflag + 2048, tag);
            const int jj = part * 8 + et, rr = lane & 15;
            const char* fpn = ws + WS_F + 2048 + (((rr & 7) | ((jj & 3) << 3)) * 8 + ((rr >> 3) | ((jj >> 2) << 1))) * 8;
            bars(4);
            const u64 v = ld_tag(fpn);
            bar();
            tags_acquire(fpn, v, tag);
        } else bars(5);
        bar();
#pragma unroll
        for (int h = 0; h < 2; ++h)
#pragma unroll
            for (int hf = 0; hf < 2; ++hf) {
                ca[h][hf] = (lds_cptr)lds + M_G0 + h * 2 * M_GS + (gm * 64 + c) * 128 + (((4 * hf + g4) ^ ((c >> 1) & 7)) << 4);
                fp[h][hf] = lp + M_G0 + h * 2 * M_GS + M_H + gn * 8192 + hf * 1024; PIN(ca[h][hf]); PIN(fp[h][hf]);
            }
      }
        G_RUN(false, 8, OFF_2K, OFF_2K, bar());
      { LANE_VARS();
        __builtin_amdgcn_s_setprio(3);
#pragma unroll
        for (int m = 0; m < 4; ++m) {
            float gy = *(const LAS float*)(lds + 16384 + (gm * 64 + m * 16 + c) * 8);
            if (part) gy = -gy;
#pragma unroll
            for (int e = 0; e < 2; ++e) {
                const int off = ((gm * 2 + (m >> 1)) * 2 + e) * 2048 + (g4 >> 1) * 1024 + ((g4 & 1) * 32 + (m & 1) * 16 + c) * 16;
                const u32x4 v = pack44(y[2 * e][m], y[2 * e + 1][m], gy);
                if (gn == part) *(LAS u32x4*)(lds + off) = v; else ST16(ws + WS_Y + (size_t)blockIdx.x * 16384 + off, v);
            }
            bar();
        }
        bars(2);
        asm volatile("s_waitcnt vmcnt(0) lgkmcnt(0)" ::: "memory");
        bar();
        if (g == 0) {
            if (lane == 0) st_tag(myflag + 6144, tag);
            const char* fpn = ws + WS_F + 6144 + (blockIdx.x ^ 4) * 8;
            bars(3);
            const u64 v = ld_tag(fpn);
            bar();
            tags_acquire(fpn, v, tag);
            bar();
        }
      }
    } else {
        LANE_VARS();
        const u64 tag = call_tag(ws);
        {
            const int wm = w >> 1, wn = w & 1;
            lds_cptr xa[2][2], tq[2][2], tv[2];
            { const int q4 = c >> 2, p4 = c & 3;
#pragma unroll
              for (int i = 0; i < 2; ++i) {
                  const int tbo = (8 * g4 + q4) * 128 + ((((wn * 4 + i * 2) ^ ((q4 >> 1) << 2) ^ ((g4 & 1) << 1)) + (p4 >> 1)) << 4) + (p4 & 1) * 8;
                  tv[i] = (lds_cptr)lds + tbo; PIN(tv[i]);
#pragma unroll
                  for (int h = 0; h < 2; ++h) {
                      xa[h][i] = (lds_cptr)lds + M_G0 + h * 2 * M_GS + wm * 4096 + sg0 * 128 + (((4 * i + g4) ^ rswz(sg0)) << 4); PIN(xa[h][i]);
                      tq[h][i] = (lds_cptr)lds + M_G0 + h * 2 * M_GS + M_H + tbo; PIN(tq[h][i]);
                  } } }
            f32x4 acc[6][2];
            bf16x8 xA[2], xB[2], fb[6];
#pragma unroll
            for (int b = 0; b < 6; ++b) { acc[b][0] = (f32x4){}; acc[b][1] = (f32x4){}; }
            xB[0] = xB[1] = fb[3] = fb[4] = fb[5] = (bf16x8){};
#define Q_MM(b_, XF, m_) acc[b_][m_] = (b_) < 4 ? MFMA16(fb[b_], XF[m_], acc[b_][m_]) : MFMA16(XF[m_], fb[b_], acc[b_][m_])
#define Q_STEP(XP, XC, ST, HF) do { \
                _Pragma("unroll") for (int s_ = 0; s_ < 6; ++s_) { \
                    _Pragma("unroll") for (int m = 0; m < 2; ++m) { \
                        if (s_ < 3) Q_MM(s_ + 3, XP, m); else Q_MM(s_ - 3, XC, m); \
                        SBAR(); \
                        if (s_ == 0) XC[m] = ld128(xa[(ST) >> 1][HF] + ((ST) & 1) * M_GS + m * 512); \
                        { const int gb_ = s_ == 0 ? (m == 0 ? 0 : -1) : (s_ < 3 ? 2 * s_ - 1 + m : (s_ == 3 && m == 0 ? 5 : -1)); \
                          if (gb_ >= 0) fb[gb_ < 0 ? 0 : gb_] = ldtr((HF) * 4096 + (gb_ < 4 ? tq[(ST) >> 1][gb_ & 1] + ((ST) & 1) * M_GS + (gb_ >> 1) * 8192 \
                                                                                           : tv[gb_ & 1] + (ST) * M_KS)); } \
                        SBAR(); } } } while (0)
#define Q_ITER(st) do { bar0(); Q_STEP(xB, xA, st, 0); Q_STEP(xA, xB, st, 1); } while (0)
            LGKM0();
            for (int it = 0; it < 4; ++it) { Q_ITER(0); Q_ITER(1); Q_ITER(2); Q_ITER(3); }
            bar0();
#pragma unroll
            for (int b = 3; b < 6; ++b) { Q_MM(b, xB, 0); Q_MM(b, xB, 1); }
            DIAG_MARK(0);
            const int hd = 2 * j + wn, t32 = rt * 4 + wm;
            const float qs = 0.17677669529663687f * 1.4426950408889634f;
            const int fo = (g4 >> 1) * 1024 + (g4 & 1) * 512;
            char* qp = ws + WS_Q + (size_t)(hd * 64 + t32) * 2048 + fo;
            char* kp = ws + WS_KV + (size_t)(hd * 64 + t32) * 2048 + fo;
#pragma unroll
            for (int a = 0; a < 2; ++a) ST16(kp + (16 * (c >> 3) + 8 * a + (c & 7)) * 16, pack44(acc[2][a], acc[3][a], 1.f));
            bar();
#pragma unroll
            for (int a = 0; a < 2; ++a) ST16(kp + (4u << 20) + (16 * a + swap23(c)) * 16, pack44(acc[4 + a][0], acc[4 + a][1], 1.f));
            bar();
#pragma unroll
            for (int a = 0; a < 2; ++a) ST16(qp + (sg0 + 4 * a) * 16, pack44(acc[0][a], acc[1][a], qs));
        }
        bars(3);
        drain();
        bar();
        if (w == 0) {
            if (lane == 0) st_tag(myflag, tag);
            const int jj = head >> 1, rr = lane & 15;
            const int pb = lane < 16 ? ((rr & 7) | ((jj & 3) << 3)) * 8 + ((rr >> 3) | ((jj >> 2) << 1)) : (lane < 32 ? tt * 16 + rr : qb * 32 + (lane - 32));
            const char* fpn = ws + WS_F + pb * 8;
            bars(4);
            const u64 v = ld_tag(fpn);
            bars(2);
            tags_acquire(fpn, v, tag);
        } else bars(6);
        bar();
        const int qt = qb * 8 + w;
        bf16x8 qf[2]; float omg;
        { const char* qp = ws + WS_Q + (size_t)(head * 64 + qt) * 2048 + lane * 16;
          asm volatile("global_load_dwordx4 %0, %1, off sc1" : "=v"(qf[0]) : "v"(qp) : "memory");
          asm volatile("global_load_dwordx4 %0, %1, off offset:1024 sc1" : "=v"(qf[1]) : "v"(qp) : "memory");
          asm volatile("global_load_dword %0, %1, off offset:4 sc1" : "=v"(omg) : "v"(ws + WS_G + (size_t)(qt * 32 + r) * 8) : "memory"); }
        bf16x8 sel; { const short one = (((lane & 1) == ((lane >> 4) & 1))) ? (short)0x3f80 : (short)0;
#pragma unroll
            for (int i = 0; i < 8; ++i) sel[i] = one; }
        f32x16 o = (f32x16){}; f32x4 l4 = (f32x4){};
        f32x16 negm, sA, sB;
        bf16x8 kf[2], vf0, vf1;
        u32x4 p0, p1;
        bars(2);
        asm volatile("s_waitcnt vmcnt(0)" : "+v"(qf[0]), "+v"(qf[1]), "+v"(omg) :: "memory");
        DIAG_MARK(1);
        kf[0] = ld128(lp + 3 * M_KS); kf[1] = ld128(lp + 3 * M_KS + 2048);
        sA = MFMA32(kf[0], qf[0], (f32x16){}); sA = MFMA32(kf[1], qf[1], sA);
        { float m = sA[0];
#pragma unroll
          for (int i = 1; i < 16; ++i) m = fmaxf(m, sA[i]);
          m = fmaxf(m, __shfl_xor(m, 32));
#pragma unroll
          for (int i = 0; i < 16; ++i) { negm[i] = -m; sA[i] -= m; } }
        kf[0] = ld128(lp + 3 * M_KS + 4096); kf[1] = ld128(lp + 3 * M_KS + 6144);
        vf0 = ld128(lp + 3 * M_KS + 1024);
        vf1 = vf0; p1 = (u32x4){0u, 0u, 0u, 0u};
#define EN(S, i, n) do { _Pragma("unroll") for (int e_ = 0; e_ < n; ++e_) S[i + e_] = EX2(S[i + e_]); } while (0)
#define A_STEP(SC, SN, v1o, kn, v0n) do { \
            SN = MFMA32(kf[0], qf[0], negm); PIN(SN); SBAR(); \
            EN(SC, 0, 3); PIN(SC); SBAR(); \
            o = MFMA32(vf1, PB(p1), o); PIN(o); SBAR(); \
            EN(SC, 3, 3); PIN(SC); SBAR(); \
            SN = MFMA32(kf[1], qf[1], SN); PIN(SN); SBAR(); \
            vf1 = ld128(lp + (v1o)); kf[0] = ld128(lp + (kn)); kf[1] = ld128(lp + (kn) + 2048); SBAR(); \
            EN(SC, 6, 2); p0[0] = cvtpk(SC[0], SC[1]); p0[1] = cvtpk(SC[2], SC[3]); PIN(SC); PIN(p0); SBAR(); \
            l4 = MFMA16(sel, PB(p1), l4); PIN(l4); SBAR(); \
            p0[2] = cvtpk(SC[4], SC[5]); p0[3] = cvtpk(SC[6], SC[7]); PIN(p0); SBAR(); EN(SC, 8, 2); PIN(SC); SBAR(); \
            o = MFMA32(vf0, PB(p0), o); PIN(o); SBAR(); \
            vf0 = ld128(lp + (v0n)); SBAR(); \
            EN(SC, 10, 3); PIN(SC); SBAR(); \
            l4 = MFMA16(sel, PB(p0), l4); PIN(l4); SBAR(); \
            EN(SC, 13, 3); \
            _Pragma("unroll") for (int i = 0; i < 4; ++i) p1[i] = cvtpk(SC[8 + 2 * i], SC[9 + 2 * i]); \
            PIN(p1); SBAR(); \
        } while (0)
#define A_ITER(st) do { \
            constexpr int b0 = (st) * M_KS; \
            bar(); \
            A_STEP(sA, sB, b0 + 1024, b0, b0 + 3072); \
            A_STEP(sB, sA, b0 + 5120, b0 + 4096, b0 + 7168); \
            } while (0)
        static_assert(NA % 4 == 0 && NY % 4 == 0 && NO % 4 == 0, "slot phase");
        LGKM0();
        for (int it = 0; it < 8; ++it) { A_ITER(0); A_ITER(1); A_ITER(2); A_ITER(3); }
        bar();
        o = MFMA32(vf1, PB(p1), o); l4 = MFMA16(sel, PB(p1), l4);
        DIAG_MARK(2);
        float l = ((lane >> 4) & 1) ? l4[1] : l4[0];
        bool ok = l < 1.0e30f;
#pragma unroll
        for (int i = 0; i < 16; ++i) ok = ok && (fabsf(o[i]) < 3.0e38f);
        if (!__all(ok)) {
            float m = -INFINITY, ls = 0.f; o = (f32x16){};
            const char* gp = kb + lane * 16;
            for (int t = 0; t < 64; ++t) {
                bf16x8 k0, k1, v0, v1;
                asm volatile("global_load_dwordx4 %0, %4, off sc1\n\tglobal_load_dwordx4 %1, %4, off offset:1024 sc1\n\t"
                             "global_load_dwordx4 %2, %5, off sc1\n\tglobal_load_dwordx4 %3, %5, off offset:1024 sc1\n\ts_waitcnt vmcnt(0)"
                             : "=&v"(k0), "=&v"(k1), "=&v"(v0), "=&v"(v1) : "v"(gp + t * 2048), "v"(gp + (4 << 20) + t * 2048) : "memory");
                f32x16 s = MFMA32(k0, qf[0], (f32x16){}); s = MFMA32(k1, qf[1], s);
                float tm = s[0];
#pragma unroll
                for (int i = 1; i < 16; ++i) tm = fmaxf(tm, s[i]);
                tm = fmaxf(tm, __shfl_xor(tm, 32));
                const float mn = fmaxf(m, tm), al = EX2(m - mn);
                m = mn; ls *= al;
#pragma unroll
                for (int i = 0; i < 16; ++i) { o[i] *= al; s[i] = EX2(s[i] - mn); ls += s[i]; }
                u32x4 a0, a1;
#pragma unroll
                for (int i = 0; i < 4; ++i) { a0[i] = cvtpk(s[2 * i], s[2 * i + 1]); a1[i] = cvtpk(s[8 + 2 * i], s[9 + 2 * i]); }
                o = MFMA32(v0, PB(a0), o); o = MFMA32(v1, PB(a1), o);
            }
            l = ls + __shfl_xor(ls, 32);
        }
        { const float sc = omg / l;
          char* op = ws + WS_O + ((size_t)qt * 64 + 2 * head) * 1024 + lane * 16;
#pragma unroll
          for (int s = 0; s < 2; ++s) ST16(op + s * 1024, pack8(o, s, sc)); }
        bars(2);
        drain();
        bar();
        if (w == 0) {
            if (lane == 0) st_tag(myflag + 4096, tag);
            const int hd = lane & 31;
            const char* fpn = ws + WS_F + 4096 + (((hd & 3) * 8 + (tt >> 1)) * 8 + (hd >> 2)) * 8;
            bars(4);
            const u64 v = ld_tag(fpn);
            bars(2);
            tags_acquire(fpn, v, tag);
        } else bars(6);
        bars(2);
        DIAG_MARK(3);
        {
            const int wm = w >> 1, wn = w & 1;
            const int q4 = (lane & 15) >> 2, p4 = lane & 3, cg = (lane >> 4) & 1, pi4 = ((p4 & 1) << 1) | (p4 >> 1);
            lds_cptr ap[2], bp[2];
#pragma unroll
            for (int h = 0; h < 2; ++h) {
                ap[h] = lp + M_G0 + h * 2 * M_GS + wm * 4096;
                bp[h] = (lds_cptr)lds + M_G0 + h * 2 * M_GS + M_H + (8 * hh + q4) * 128 + ((wn ^ (q4 >> 1)) << 6) + cg * 32 + pi4 * 8;
            }
            f32x16 acc = (f32x16){};
            bf16x8 a0[4], b0[4], a1[4], b1[4];
#pragma unroll
            for (int i = 0; i < 4; ++i) { a1[i] = (bf16x8){}; b1[i] = (bf16x8){}; }
#define F_ITER(st, AP, BP, AC, BC) do { \
                bar0(); \
                _Pragma("unroll") for (int i = 0; i < 4; ++i) { \
                    AC[i] = ld128(ap[(st) >> 1] + ((st) & 1) * M_GS + i * 1024); BC[i] = ldtr(bp[(st) >> 1] + ((st) & 1) * M_GS + i * 2048); SBAR(); \
                    acc = MFMA32(BP[i], AP[i], acc); PIN(acc); SBAR(); } \
                } while (0)
            LGKM0();
            for (int it = 0; it < 4; ++it) { F_ITER(0, a1, b1, a0, b0); F_ITER(1, a0, b0, a1, b1); F_ITER(2, a1, b1, a0, b0); F_ITER(3, a0, b0, a1, b1); }
#pragma unroll
            for (int i = 0; i < 4; ++i) acc = MFMA32(b1[i], a1[i], acc);
            DIAG_MARK(4);
#pragma unroll
            for (int s = 0; s < 2; ++s) {
                const u32x4 ya = *(const LAS u32x4*)(lp + w * 2048 + s * 1024), yb = *(const LAS u32x4*)(lp + 16384 + w * 2048 + s * 1024);
#pragma unroll
                for (int i = 0; i < 4; ++i) {
                    acc[8 * s + 2 * i] += __uint_as_float(ya[i] << 16) + __uint_as_float(yb[i] << 16);
                    acc[8 * s + 2 * i + 1] += __uint_as_float(ya[i] & 0xffff0000u) + __uint_as_float(yb[i] & 0xffff0000u);
                }
            }
            u16* op = out + (size_t)(tt * 128 + wm * 32 + r) * 1024 + (2 * et + part) * 64 + wn * 32 + hh * 8;
#pragma unroll
            for (int s = 0; s < 2; ++s) ST16(op + s * 16, pack8(acc, s, 1.f));
        }
        if (blockIdx.x == 0 && w == 0 && lane == 0) st_tag(ws + WS_E, tag);
        DIAG_END(0, ws);
    }
}

extern "C" void kernel_launch(void* const* d_in, const int* in_sizes, int n_in, void* d_out, int out_size, void* d_ws, size_t ws_size,
                              hipStream_t stream) {
    (void)hipFuncSetAttribute((const void*)k_all, hipFuncAttributeMaxDynamicSharedMemorySize, M_LDS);
    k_all<<<256, 1024, M_LDS, stream>>>((const u16*)d_in[0], (const u16*)d_in[1], (const u16*)d_in[2], (const u16*)d_in[3], (const u16*)d_in[4],
                                        (const u16*)d_in[6], (const u16*)d_in[7], (const u16*)d_in[8], (char*)d_ws, (u16*)d_out);
}
```

```cpp
#include <hip/hip_runtime.h>
#include <hip/hip_bf16.h>

typedef unsigned short u16;
typedef short bf16x8 __attribute__((ext_vector_type(8)));
typedef short s16x4 __attribute__((ext_vector_type(4)));
typedef float f32x16 __attribute__((ext_vector_type(16)));
typedef float f32x4 __attribute__((ext_vector_type(4)));
typedef float f32x2 __attribute__((ext_vector_type(2)));
typedef unsigned u32x4 __attribute__((ext_vector_type(4)));
#define LAS __attribute__((address_space(3)))
typedef LAS const char* lds_cptr;

constexpr size_t WS_Q = 0;
constexpr size_t WS_KV = 4u << 20;
constexpr size_t WS_AB = 12u << 20;
constexpr size_t WS_O = 20u << 20;
constexpr size_t WS_Y = 24u << 20;
constexpr size_t WS_G = 40u << 20;
constexpr size_t WS_F = WS_G + 65536;
constexpr size_t WS_E = WS_F + 16384;

#define MFMA32(a, b, c) __builtin_amdgcn_mfma_f32_32x32x16_bf16((a), (b), (c), 0, 0, 0)
#define MFMA16(a, b, c) __builtin_amdgcn_mfma_f32_16x16x32_bf16((a), (b), (c), 0, 0, 0)
#define SBAR() __builtin_amdgcn_sched_barrier(0)
#define PIN(x) asm volatile("" : "+v"(x))
#define EX2(v) __builtin_amdgcn_exp2f(v)
#define PB(p) __builtin_bit_cast(bf16x8, p)
#define LGKM0() __builtin_amdgcn_s_waitcnt(0xC07F)

__device__ __forceinline__ unsigned cvtpk(float lo, float hi) {
    typedef __bf16 bf16x2_t __attribute__((ext_vector_type(2)));
    return __builtin_bit_cast(unsigned, __builtin_convertvector((f32x2){lo, hi}, bf16x2_t)); }
__device__ __forceinline__ float bfr(float f) { return __uint_as_float(cvtpk(f, 0.f) << 16); }

#define DSC " sc1"
__device__ __forceinline__ void glds16d(unsigned voff, const void* sbase, unsigned ldsdst) {
    asm volatile("s_mov_b32 m0, %2\n\ts_nop 0\n\tglobal_load_lds_dwordx4 %0, %1" DSC :: "v"(voff), "s"(sbase), "s"(ldsdst) : "memory");
}
__device__ __forceinline__ void glds2d(unsigned v0, unsigned v1, const void* sbase, unsigned ldsdst) {
    asm volatile("s_mov_b32 m0, %3\n\ts_nop 0\n\tglobal_load_lds_dwordx4 %0, %2" DSC "\n\tglobal_load_lds_dwordx4 %1, %2 offset:1024" DSC
                 :: "v"(v0), "v"(v1), "s"(sbase), "s"(ldsdst) : "memory");
}
__device__ __forceinline__ void glds4d(unsigned v0, unsigned v1, unsigned v2, unsigned v3, const void* sbase, unsigned ldsdst) {
    asm volatile("s_mov_b32 m0, %5\n\ts_nop 0\n\tglobal_load_lds_dwordx4 %0, %4" DSC "\n\tglobal_load_lds_dwordx4 %1, %4 offset:1024" DSC "\n\t"
                 "global_load_lds_dwordx4 %2, %4 offset:2048" DSC "\n\tglobal_load_lds_dwordx4 %3, %4 offset:3072" DSC
                 :: "v"(v0), "v"(v1), "v"(v2), "v"(v3), "s"(sbase), "s"(ldsdst) : "memory");
}
__device__ __forceinline__ void glds2(unsigned v0, unsigned v1, const void* sbase, unsigned ldsdst) {
    asm volatile("s_mov_b32 m0, %3\n\ts_nop 0\n\tglobal_load_lds_dwordx4 %0, %2\n\tglobal_load_lds_dwordx4 %1, %2 offset:1024"
                 :: "v"(v0), "v"(v1), "s"(sbase), "s"(ldsdst) : "memory");
}
__device__ __forceinline__ void glds4(unsigned v0, unsigned v1, unsigned v2, unsigned v3, const void* sbase, unsigned ldsdst) {
    asm volatile("s_mov_b32 m0, %5\n\ts_nop 0\n\tglobal_load_lds_dwordx4 %0, %4\n\tglobal_load_lds_dwordx4 %1, %4 offset:1024\n\t"
                 "global_load_lds_dwordx4 %2, %4 offset:2048\n\tglobal_load_lds_dwordx4 %3, %4 offset:3072"
                 :: "v"(v0), "v"(v1), "v"(v2), "v"(v3), "s"(sbase), "s"(ldsdst) : "memory");
}
template <int N> __device__ __forceinline__ void wait_bar() {
    SBAR();
    asm volatile("s_waitcnt vmcnt(%0)\n\ts_barrier" :: "n"(N) : "memory");
    SBAR();
}
__device__ __forceinline__ void bar() { SBAR(); asm volatile("s_barrier" ::: "memory"); SBAR(); }
__device__ __forceinline__ void bar0() { SBAR(); asm volatile("s_waitcnt lgkmcnt(0)\n\ts_barrier" ::: "memory"); SBAR(); }
__device__ __forceinline__ bf16x8 ld128(lds_cptr p) { return *(const LAS bf16x8*)p; }
__device__ __forceinline__ bf16x8 ldtr(lds_cptr p) {
    s16x4 lo = __builtin_amdgcn_ds_read_tr16_b64_v4i16((LAS s16x4*)p);
    s16x4 hi = __builtin_amdgcn_ds_read_tr16_b64_v4i16((LAS s16x4*)(p + 512));
    return (bf16x8){lo[0], lo[1], lo[2], lo[3], hi[0], hi[1], hi[2], hi[3]};
}
__device__ __forceinline__ u32x4 pack8(const f32x16& a, int s, float sc) {
    u32x4 w;
    w[0] = cvtpk(a[8 * s + 0] * sc, a[8 * s + 1] * sc); w[1] = cvtpk(a[8 * s + 2] * sc, a[8 * s + 3] * sc);
    w[2] = cvtpk(a[8 * s + 4] * sc, a[8 * s + 5] * sc); w[3] = cvtpk(a[8 * s + 6] * sc, a[8 * s + 7] * sc);
    return w;
}
#define STF " sc1"
#define ST16(p, v) do { const u32x4 v_ = (v); asm volatile("global_store_dwordx4 %0, %1, off" STF "\n\ts_nop 1" :: "v"(p), "v"(v_) : "memory"); } while (0)
__device__ __forceinline__ int swap23(int i) { return (i & ~12) | ((i & 4) << 1) | ((i & 8) >> 1); }

__device__ __forceinline__ int lf_order(int tau, int a, int c, int G) {
    if (tau == 0) return a;
    const int g = (tau - 1) >> 2, pos = (tau - 1) & 3;
    if (g != G - 1 && pos == c) return 4 * (g + 1) + a;
    const int b = (g == G - 1 || pos < c) ? pos : pos - 1;
    return 4 * g + (b < a ? b : b + 1);
}

__device__ __forceinline__ int rswz(int row) { return ((row >> 1) & 1) | (((row >> 3) & 3) << 1); }
__device__ __forceinline__ u32x4 pack44(const f32x4& a, const f32x4& b, float sc) {
    return (u32x4){cvtpk(a[0] * sc, a[1] * sc), cvtpk(a[2] * sc, a[3] * sc), cvtpk(b[0] * sc, b[1] * sc), cvtpk(b[2] * sc, b[3] * sc)};
}
__device__ __forceinline__ void drain() { asm volatile("s_waitcnt vmcnt(0)" ::: "memory"); }
typedef unsigned long long u64;
__device__ __forceinline__ u64 ld_tag(const char* p) { return __hip_atomic_load((const u64*)p, __ATOMIC_RELAXED, __HIP_MEMORY_SCOPE_AGENT); }
__device__ __forceinline__ void st_tag(char* p, u64 v) { __hip_atomic_store((u64*)p, v, __ATOMIC_RELAXED, __HIP_MEMORY_SCOPE_AGENT); }
__device__ __forceinline__ void tags_acquire(const char* p, u64 v, u64 tag) {
#pragma nounroll
    for (int spin = 0; !__all(v == tag) && spin < (1 << 22); ++spin) { __builtin_amdgcn_s_sleep(2); v = ld_tag(p); }
    asm volatile("" ::: "memory");
}
__device__ __forceinline__ u64 call_tag(const char* ws) {
    const u64 e = ld_tag(ws + WS_E) + 1;
    return ((u64)(unsigned)__builtin_amdgcn_readfirstlane((int)(e >> 32)) << 32) | (unsigned)__builtin_amdgcn_readfirstlane((int)e);
}
__device__ __forceinline__ int fresh_lane() {
    int l; asm volatile("v_mbcnt_lo_u32_b32 %0, -1, 0\n\tv_mbcnt_hi_u32_b32 %0, -1, %0" : "=v"(l)); return l;
}
__device__ __forceinline__ float DOT2(unsigned a, unsigned b, float acc) {
    typedef __bf16 bf2_t __attribute__((ext_vector_type(2)));
    return __builtin_amdgcn_fdot2_f32_bf16(__builtin_bit_cast(bf2_t, a), __builtin_bit_cast(bf2_t, b), acc, false);
}
__device__ __forceinline__ void bars(int n) { for (int i = 0; i < n; ++i) bar(); }

constexpr int M_KS = 8192, M_G0 = 4 * M_KS, M_GS = 32768, M_H = 16384, M_LDS = M_G0 + 4 * M_GS;
constexpr int CODE_KB = 23;
constexpr int NA = 32, NY = 44, NO = 76;
__global__ __launch_bounds__(1024, 4) void k_all(const u16* __restrict__ x, const u16* __restrict__ cr, const u16* __restrict__ ci,
                                                  const u16* __restrict__ hr, const u16* __restrict__ hi, const u16* __restrict__ wqkv,
                                                  const u16* __restrict__ wo, const u16* __restrict__ gw, char* ws, u16* __restrict__ out) {
    extern __shared__ __attribute__((aligned(16))) char smem[];
    LAS char* lds = (LAS char*)smem;
    unsigned long long pc0; asm volatile("s_getpc_b64 %0" : "=s"(pc0));
    const int w = (__builtin_amdgcn_readfirstlane(threadIdx.x >> 6) + 12) & 15;
    const int xcd = blockIdx.x & 7, idx = blockIdx.x >> 3;
    const int rt = (xcd & 1) * 8 + (idx & 7), j = (xcd >> 1) * 4 + (idx >> 3);
    const int head = xcd * 4 + (idx >> 3), qb = idx & 7;
    const int part = xcd >> 2, tt = ((xcd >> 1) & 1) * 8 + (idx & 7), et = (xcd & 1) * 4 + (idx >> 3);
    const char* kb = ws + WS_KV + (size_t)head * 131072;
#define LANE_VARS() const int lane = fresh_lane(), r = lane & 31, hh = lane >> 5, c = lane & 15, g4 = lane >> 4, sg0 = 8 * (c >> 2) + (c & 3); \
    lds_cptr lp = (lds_cptr)lds + lane * 16; (void)r; (void)hh; (void)g4; (void)sg0; (void)lp
    char* myflag = ws + WS_F + blockIdx.x * 8;
    if (w >= 12) {
        LANE_VARS();
        const int g = w - 12;
        const int la = ((idx >> 3) - (idx & 7)) & 3, lc = (idx >> 1) & 3;
        const int tab16 = lf_order(lane & 15, la, lc, 4), tab32 = lf_order(lane & 31, la, lc, 8);
        const unsigned lds0 = (unsigned)(uintptr_t)lds;
        const unsigned vL = (unsigned)(lane * 16);
        unsigned vR[4], vW[2], vC[4], vA[2], vT[2];
#pragma unroll
        for (int i = 0; i < 4; ++i) {
            vR[i] = (unsigned)((8 * i + (lane >> 3)) * 2048 + (((lane & 7) ^ rswz(8 * i + (lane >> 3))) << 4) - i * 1024);
            vC[i] = (unsigned)((8 * i + (lane >> 3)) * 4096 + (((lane & 7) ^ ((4 * i + (lane >> 4)) & 7)) << 4) - i * 1024);
        }
#pragma unroll
        for (int i = 0; i < 2; ++i) {
            vW[i] = (unsigned)((8 * i + (lane >> 3)) * 6144 + (((lane & 7) ^ (((lane >> 4) & 1) << 2) ^ (i << 1)) << 4) - i * 1024);
            vT[i] = (unsigned)((8 * i + (lane >> 3)) * 2048 + (((lane & 7) ^ (((lane >> 4) & 1) << 2)) << 4) - i * 1024);
            vA[i] = (unsigned)(lane * 16 + i * (65536 - 2048));
        }
        const unsigned vKV = (unsigned)(lane * 16 + (4 << 20) - 3072 - 1024);
        const char* xb = (const char*)x + (size_t)(rt * 128 + g * 32) * 2048;
        const char* hb = (const char*)(j < 8 ? hr : hi) + (size_t)((j & 7) * 128 + g * 32) * 2048;
        const char* wb = (const char*)wqkv + (size_t)g * 16 * 6144 + j * 128;
        const char* cb = (const char*)(part ? ci : cr) + (size_t)(tt * 128 + 32 * g) * 4096;
        const char* ab = ws + WS_AB + (size_t)(part * 64 + et * 8 + 2 * g) * 65536;
        const char* ob = ws + WS_O + (size_t)(tt * 4 + g) * 65536;
        const char* wob = (const char*)wo + (2 * et + part) * 128;
        const unsigned dG = lds0 + M_G0 + g * 4096, dK = lds0 + g * 2048;
        auto sP = [&](int n) {
            const int kp = __builtin_amdgcn_readlane(tab16, n); const unsigned so = (n & 3) * M_GS;
            const char* b = wb + (size_t)(kp * (64 * 6144));
            glds4(vR[0], vR[1], vR[2], vR[3], xb + kp * 128, dG + so);
            glds2(vW[0], vW[1], b, dG + so + M_H - g * 2048); glds2(vW[0], vW[1], b + 2048, dG + so + M_H + 8192 - g * 2048);
            glds2(vW[0], vW[1], b + 4096, dK + (n & 3) * M_KS);
        };
        auto sH = [&](int n) {
            const int kp = __builtin_amdgcn_readlane(tab16, n & 15); const unsigned so = (n & 3) * M_GS;
            glds4(vR[0], vR[1], vR[2], vR[3], xb + kp * 128, dG + so); glds4(vR[0], vR[1], vR[2], vR[3], hb + kp * 128, dG + so + M_H);
        };
        auto sKV = [&](int n) {
            glds2d(vL, vKV, kb + ((n - NA) * 4096 + 4096 + g * 1024), dK + (n & 3) * M_KS);
        };
        auto sY = [&](int n) {
            const int kp = __builtin_amdgcn_readlane(tab32, n - NY); const unsigned so = (n & 3) * M_GS;
            glds4(vC[0], vC[1], vC[2], vC[3], cb + kp * 128, dG + so); glds4d(vA[0], vA[0], vA[1], vA[1], ab + kp * 2048, dG + so + M_H);
        };
        auto sO = [&](int n) {
            const int kp = __builtin_amdgcn_readlane(tab16, n - NO); const unsigned so = (n & 3) * M_GS;
            glds2(vT[0], vT[1], wob + (size_t)((kp * 64 + g * 16) * 2048), dG + so + M_H - g * 2048);
            glds4d(vL, vL, vL, vL, ob + kp * 4096, dG + so);
        };
#define WB(N) wait_bar<N>()
        auto sC = [&](int n) { const int kp = __builtin_amdgcn_readlane(tab32, n - NY); glds4(vC[0], vC[1], vC[2], vC[3], cb + kp * 128, dG + (n & 3) * M_GS); };
        auto sAB = [&](int n) { const int kp = __builtin_amdgcn_readlane(tab32, n - NY); glds4d(vA[0], vA[0], vA[1], vA[1], ab + kp * 2048, dG + (n & 3) * M_GS + M_H); };
        sP(0); sP(1); WB(10);
        sP(2);
        for (int k = 1; k < 14; ++k) { sP(k + 2); WB(20); }
        sH(16); WB(18);
        for (int k = 15; k < 30; ++k) { sH(k + 2); WB(16); }
        glds16d(vL, kb + g * 1024, dK + 3 * M_KS); glds16d(vL, kb + (4 << 20), dK + 3 * M_KS + 1024);
        WB(10);
        for (int k = 31; k < 36; ++k) { sKV(k + 1); WB(2); }
        sKV(37); sC(44); WB(6);
        for (int k = 37; k < 40; ++k) { sKV(k + 1); sC(k + 8); WB(10); }
        sKV(41); WB(6);
        sKV(42); WB(2);
        sKV(43); WB(2);
        sKV(44); sAB(44); sAB(45); WB(10);
        sKV(45); sAB(46); WB(10);
        sKV(46); sAB(47); WB(10);
        sKV(47); sY(48); WB(14);
        for (int k = 47; k < 62; ++k) { sKV(k + 1); sY(k + 2); WB(18); }
        { const int vo = g < 3 ? 31 * 4096 + 1024 + g * 1024 : 0;
          glds16d(vL, kb + (4 << 20) + vo, dK + 3 * M_KS + 1024); sY(64); WB(17); }
        for (int k = 63; k < 70; ++k) { sY(k + 2); WB(16); }
        glds16d(vL, ws + WS_G + (size_t)tt * 1024, lds0 + 16384);
        sY(72); WB(17);
        sY(73); WB(17);
        sY(74); WB(16);
        sY(75); WB(16);
        WB(8);
        sO(76);
        for (int k = 75; k < 89; ++k) { sO(k + 2); WB(12); }
        sO(91); glds4d(vL, vL, vL, vL, ws + WS_Y + (size_t)(blockIdx.x ^ 4) * 16384 + g * 4096, lds0 + 16384 + g * 4096);
        WB(16);
        WB(10);
        WB(0);
        static_assert(NA == 32 && NY == 44 && NO == 76, "the DMA program is written for this schedule");
    } else if (w >= 8) {
        LANE_VARS();
        const int g = w - 8, gm = g >> 1, gn = g & 1;
        { const int piece = (idx & 7) * 4 + g;
          if (piece < CODE_KB) { u32x4 v; asm volatile("global_load_dwordx4 %0, %1, %2\n\ts_waitcnt vmcnt(0)" : "=&v"(v) : "v"(piece * 1024 + lane * 16), "s"(pc0 & ~255ull) : "memory"); } }
        const u64 tag = call_tag(ws);
        __builtin_amdgcn_s_setprio(3);
        bars(2);
        {
            const u32x4* xr = (const u32x4*)(x + (size_t)(blockIdx.x * 8 + 2 * g) * 1024); const u32x4* gr = (const u32x4*)gw;
            const u32x4 a0 = xr[lane], a1 = xr[lane + 64], a2 = xr[lane + 128], a3 = xr[lane + 192], b0 = gr[lane], b1 = gr[lane + 64];
            bars(4);
            float z0 = 0.f, z1 = 0.f;
#pragma unroll
            for (int e = 0; e < 4; ++e) { z0 = DOT2(a0[e], b0[e], z0); z1 = DOT2(a2[e], b0[e], z1); z0 = DOT2(a1[e], b1[e], z0); z1 = DOT2(a3[e], b1[e], z1); }
#define DPP_ADD(v, ctrl, rmask) v += __uint_as_float(__builtin_amdgcn_update_dpp(0u, __float_as_uint(v), ctrl, rmask, 0xf, false))
            DPP_ADD(z0, 0x111, 0xf); DPP_ADD(z1, 0x111, 0xf); DPP_ADD(z0, 0x112, 0xf); DPP_ADD(z1, 0x112, 0xf);
            DPP_ADD(z0, 0x114, 0xf); DPP_ADD(z1, 0x114, 0xf); DPP_ADD(z0, 0x118, 0xf); DPP_ADD(z1, 0x118, 0xf);
            DPP_ADD(z0, 0x142, 0xa); DPP_ADD(z1, 0x142, 0xa); DPP_ADD(z0, 0x143, 0xc); DPP_ADD(z1, 0x143, 0xc);
            z0 = __uint_as_float(__builtin_amdgcn_readlane(__float_as_uint(z0), 63)); z1 = __uint_as_float(__builtin_amdgcn_readlane(__float_as_uint(z1), 63));
            bar();
            const float gt = bfr(1.f / (1.f + __expf(-bfr((lane & 1) ? z1 : z0))));
            if (lane < 2) st_tag(ws + WS_G + (size_t)(blockIdx.x * 8 + 2 * g + lane) * 8, ((u64)__float_as_uint(bfr(1.f - gt)) << 32) | __float_as_uint(gt));
            bars(6);
            __builtin_amdgcn_s_setprio(0);
            bars(2);
        }
        f32x4 y[4][4];
        bf16x8 cA[4], cB[4], fa[4];
        lds_cptr ca[2][2], fp[2][2];
#define G_MM(SW, a_, CX, m_) y[a_][m_] = (SW) ? MFMA16(CX[m_], fa[a_], y[a_][m_]) : MFMA16(fa[a_], CX[m_], y[a_][m_])
#define G_STEP(SW, CP, CC, ST, HF, CO, FO) do { \
            _Pragma("unroll") for (int s_ = 0; s_ < 4; ++s_) { \
                _Pragma("unroll") for (int m = 0; m < 4; ++m) { \
                    if (s_ < 2) G_MM(SW, s_ + 2, CP, m); else G_MM(SW, s_ - 2, CC, m); \
                    if (s_ == 0) CC[m] = ld128(ca[(ST) >> 1][HF] + ((ST) & 1) * M_GS + CO(m)); \
                    if (m == 0 && s_ < 3) fa[s_] = ld128(fp[(ST) >> 1][HF] + ((ST) & 1) * M_GS + FO(s_)); \
                    if (m == 2 && s_ == 2) fa[3] = ld128(fp[(ST) >> 1][HF] + ((ST) & 1) * M_GS + FO(3)); \
                    SBAR(); } } } while (0)
#define G_ITER(SW, st, CO, FO) do { bar0(); G_STEP(SW, cB, cA, st, 0, CO, FO); G_STEP(SW, cA, cB, st, 1, CO, FO); } while (0)
#define G_RUN(SW, NIT, CO, FO, PRE) do { \
            _Pragma("unroll") for (int a = 0; a < 4; ++a) { cB[a] = (bf16x8){}; fa[a] = (bf16x8){}; \
                _Pragma("unroll") for (int b = 0; b < 4; ++b) y[a][b] = (f32x4){}; } \
            PRE; LGKM0(); \
            for (int it = 0; it < NIT; ++it) { G_ITER(SW, 0, CO, FO); G_ITER(SW, 1, CO, FO); G_ITER(SW, 2, CO, FO); G_ITER(SW, 3, CO, FO); } \
            bar0(); \
            _Pragma("unroll") for (int a = 2; a < 4; ++a) _Pragma("unroll") for (int m = 0; m < 4; ++m) G_MM(SW, a, cB, m); } while (0)
#define OFF_SIG(i) (((i) >> 1) * 4096 + ((i) & 1) * 512)
#define OFF_2K(i) ((i) * 2048)
#pragma unroll
        for (int h = 0; h < 2; ++h)
#pragma unroll
            for (int hf = 0; hf < 2; ++hf) {
                const int ra = M_G0 + h * 2 * M_GS + sg0 * 128 + (((4 * hf + g4) ^ rswz(sg0)) << 4);
                ca[h][hf] = (lds_cptr)lds + ra + gm * 8192; fp[h][hf] = (lds_cptr)lds + ra + M_H + gn * 8192; PIN(ca[h][hf]); PIN(fp[h][hf]);
            }
        G_RUN(true, 4, OFF_SIG, OFF_SIG, drain(); bar());
      { LANE_VARS();
#pragma unroll
        for (int gr = 0; gr < 2; ++gr) {
            char* abp = ws + WS_AB + ((size_t)(j * 8 + gn * 4) * 64 + rt * 4 + gm * 2 + gr) * 1024 + lane * 16;
#pragma unroll
            for (int b = 0; b < 4; ++b) ST16(abp + (size_t)b * 65536, pack44(y[b][2 * gr], y[b][2 * gr + 1], 1.f));
            bar();
        }
        bar();
        drain();
        bar();
        if (g == 0) {
            if (lane == 0) st_tag(myflag + 2048, tag);
            const int jj = part * 8 + et, rr = lane & 15;
            const char* fpn = ws + WS_F + 2048 + (((rr & 7) | ((jj & 3) << 3)) * 8 + ((rr >> 3) | ((jj >> 2) << 1))) * 8;
            bars(4);
            const u64 v = ld_tag(fpn);
            bar();
            tags_acquire(fpn, v, tag);
        } else bars(5);
        bar();
#pragma unroll
        for (int h = 0; h < 2; ++h)
#pragma unroll
            for (int hf = 0; hf < 2; ++hf) {
                ca[h][hf] = (lds_cptr)lds + M_G0 + h * 2 * M_GS + (gm * 64 + c) * 128 + (((4 * hf + g4) ^ ((c >> 1) & 7)) << 4);
                fp[h][hf] = lp + M_G0 + h * 2 * M_GS + M_H + gn * 8192 + hf * 1024; PIN(ca[h][hf]); PIN(fp[h][hf]);
            }
      }
        G_RUN(false, 8, OFF_2K, OFF_2K, bar());
      { LANE_VARS();
        __builtin_amdgcn_s_setprio(3);
#pragma unroll
        for (int m = 0; m < 4; ++m) {
            float gy = *(const LAS float*)(lds + 16384 + (gm * 64 + m * 16 + c) * 8);
            if (part) gy = -gy;
#pragma unroll
            for (int e = 0; e < 2; ++e) {
                const int off = ((gm * 2 + (m >> 1)) * 2 + e) * 2048 + (g4 >> 1) * 1024 + ((g4 & 1) * 32 + (m & 1) * 16 + c) * 16;
                const u32x4 v = pack44(y[2 * e][m], y[2 * e + 1][m], gy);
                if (gn == part) *(LAS u32x4*)(lds + off) = v; else ST16(ws + WS_Y + (size_t)blockIdx.x * 16384 + off, v);
            }
            bar();
        }
        bars(2);
        asm volatile("s_waitcnt vmcnt(0) lgkmcnt(0)" ::: "memory");
        bar();
        if (g == 0) {
            if (lane == 0) st_tag(myflag + 6144, tag);
            const char* fpn = ws + WS_F + 6144 + (blockIdx.x ^ 4) * 8;
            bars(3);
            const u64 v = ld_tag(fpn);
            bar();
            tags_acquire(fpn, v, tag);
            bar();
        }
      }
    } else {
        LANE_VARS();
        const u64 tag = call_tag(ws);
        {
            const int wm = w >> 2, wn = (w >> 1) & 1, wq = w & 1;
            lds_cptr xa[2][2], tq[2][2], tv;
            { const int q4 = c >> 2, p4 = c & 3;
#pragma unroll
              for (int i = 0; i < 2; ++i) {
                  const int tbo = (8 * g4 + q4) * 128 + ((((wn * 4 + i * 2) ^ ((q4 >> 1) << 2) ^ ((g4 & 1) << 1)) + (p4 >> 1)) << 4) + (p4 & 1) * 8;
#pragma unroll
                  for (int h = 0; h < 2; ++h) {
                      xa[h][i] = (lds_cptr)lds + M_G0 + h * 2 * M_GS + wm * 8192 + sg0 * 128 + (((4 * i + g4) ^ rswz(sg0)) << 4); PIN(xa[h][i]);
                      tq[h][i] = (lds_cptr)lds + M_G0 + h * 2 * M_GS + M_H + wq * 8192 + tbo; PIN(tq[h][i]);
                  } }
              tv = (lds_cptr)lds + (8 * g4 + q4) * 128 + ((((wn * 4 + wq * 2) ^ ((q4 >> 1) << 2) ^ ((g4 & 1) << 1)) + (p4 >> 1)) << 4) + (p4 & 1) * 8; PIN(tv); }
            f32x4 acc[3][4];
            bf16x8 xA[4], xB[4], fb[3];
#pragma unroll
            for (int m = 0; m < 4; ++m) { acc[0][m] = (f32x4){}; acc[1][m] = (f32x4){}; acc[2][m] = (f32x4){}; xB[m] = (bf16x8){}; }
            fb[1] = fb[2] = (bf16x8){};
#define Q_MM(b_, XF, m_) acc[b_][m_] = (b_) < 2 ? MFMA16(fb[b_], XF[m_], acc[b_][m_]) : MFMA16(XF[m_], fb[b_], acc[b_][m_])
#define Q_STEP(XP, XC, ST, HF) do { \
                _Pragma("unroll") for (int s_ = 0; s_ < 3; ++s_) { \
                    _Pragma("unroll") for (int m = 0; m < 4; ++m) { \
                        if (s_ < 2) Q_MM(s_ + 1, XP, m); else Q_MM(0, XC, m); \
                        SBAR(); \
                        if (s_ < 2 && m >= 2) XC[2 * s_ + m - 2] = ld128(xa[(ST) >> 1][HF] + ((ST) & 1) * M_GS + s_ * 4096 + (m - 2) * 512); \
                        if (m == 0) fb[s_] = ldtr((HF) * 4096 + (s_ < 2 ? tq[(ST) >> 1][s_ & 1] + ((ST) & 1) * M_GS : tv + (ST) * M_KS)); \
                        SBAR(); } } } while (0)
#define Q_ITER(st) do { bar0(); Q_STEP(xB, xA, st, 0); Q_STEP(xA, xB, st, 1); } while (0)
            LGKM0();
            for (int it = 0; it < 4; ++it) { Q_ITER(0); Q_ITER(1); Q_ITER(2); Q_ITER(3); }
            bar0();
#pragma unroll
            for (int b = 1; b < 3; ++b)
#pragma unroll
                for (int m = 0; m < 4; ++m) Q_MM(b, xB, m);
            const int hd = 2 * j + wn;
            const float qs = wq ? 1.f : 0.17677669529663687f * 1.4426950408889634f;
            char* bp = ws + (size_t)(hd * 64 + rt * 4 + wm * 2) * 2048 + (g4 >> 1) * 1024 + (g4 & 1) * 512;
            char* qk = bp + (wq ? WS_KV + (16 * (c >> 3) + (c & 7)) * 16 : WS_Q + sg0 * 16);
            const int as = wq ? 128 : 64;
#pragma unroll
            for (int gr = 0; gr < 2; ++gr) { ST16(bp + WS_KV + (4u << 20) + gr * 2048 + (16 * wq + swap23(c)) * 16, pack44(acc[2][2 * gr], acc[2][2 * gr + 1], 1.f)); }
            bar();
#pragma unroll
            for (int a = 0; a < 2; ++a) ST16(qk + a * as, pack44(acc[0][a], acc[1][a], qs));
            bar();
#pragma unroll
            for (int a = 0; a < 2; ++a) ST16(qk + 2048 + a * as, pack44(acc[0][2 + a], acc[1][2 + a], qs));
        }
        bars(3);
        drain();
        bar();
        if (w == 0) {
            if (lane == 0) st_tag(myflag, tag);
            const int jj = head >> 1, rr = lane & 15;
            const int pb = lane < 16 ? ((rr & 7) | ((jj & 3) << 3)) * 8 + ((rr >> 3) | ((jj >> 2) << 1)) : (lane < 32 ? tt * 16 + rr : qb * 32 + (lane - 32));
            const char* fpn = ws + WS_F + pb * 8;
            bars(4);
            const u64 v = ld_tag(fpn);
            bars(2);
            tags_acquire(fpn, v, tag);
        } else bars(6);
        bar();
        const int qt = qb * 8 + w;
        bf16x8 qf[2]; float omg;
        { const char* qp = ws + WS_Q + (size_t)(head * 64 + qt) * 2048 + lane * 16;
          asm volatile("global_load_dwordx4 %0, %1, off sc1" : "=v"(qf[0]) : "v"(qp) : "memory");
          asm volatile("global_load_dwordx4 %0, %1, off offset:1024 sc1" : "=v"(qf[1]) : "v"(qp) : "memory");
          asm volatile("global_load_dword %0, %1, off offset:4 sc1" : "=v"(omg) : "v"(ws + WS_G + (size_t)(qt * 32 + r) * 8) : "memory"); }
        bf16x8 sel; { const short one = (((lane & 1) == ((lane >> 4) & 1))) ? (short)0x3f80 : (short)0;
#pragma unroll
            for (int i = 0; i < 8; ++i) sel[i] = one; }
        f32x16 o = (f32x16){}; f32x4 l4 = (f32x4){};
        f32x16 negm, sA, sB;
        bf16x8 kf[2], vf0, vf1;
        u32x4 p0, p1;
        bars(2);
        asm volatile("s_waitcnt vmcnt(0)" : "+v"(qf[0]), "+v"(qf[1]), "+v"(omg) :: "memory");
        kf[0] = ld128(lp + 3 * M_KS); kf[1] = ld128(lp + 3 * M_KS + 2048);
        sA = MFMA32(kf[0], qf[0], (f32x16){}); sA = MFMA32(kf[1], qf[1], sA);
        { float m = sA[0];
#pragma unroll
          for (int i = 1; i < 16; ++i) m = fmaxf(m, sA[i]);
          m = fmaxf(m, __shfl_xor(m, 32));
#pragma unroll
          for (int i = 0; i < 16; ++i) { negm[i] = -m; sA[i] -= m; } }
        kf[0] = ld128(lp + 3 * M_KS + 4096); kf[1] = ld128(lp + 3 * M_KS + 6144);
        vf0 = ld128(lp + 3 * M_KS + 1024);
        vf1 = vf0; p1 = (u32x4){0u, 0u, 0u, 0u};
#define EN(S, i, n) do { _Pragma("unroll") for (int e_ = 0; e_ < n; ++e_) S[i + e_] = EX2(S[i + e_]); } while (0)
#define A_STEP(SC, SN, v1o, kn, v0n) do { \
            SN = MFMA32(kf[0], qf[0], negm); PIN(SN); SBAR(); \
            EN(SC, 0, 3); PIN(SC); SBAR(); \
            o = MFMA32(vf1, PB(p1), o); PIN(o); SBAR(); \
            EN(SC, 3, 3); PIN(SC); SBAR(); \
            SN = MFMA32(kf[1], qf[1], SN); PIN(SN); SBAR(); \
            vf1 = ld128(lp + (v1o)); kf[0] = ld128(lp + (kn)); kf[1] = ld128(lp + (kn) + 2048); SBAR(); \
            EN(SC, 6, 2); p0[0] = cvtpk(SC[0], SC[1]); p0[1] = cvtpk(SC[2], SC[3]); PIN(SC); PIN(p0); SBAR(); \
            l4 = MFMA16(sel, PB(p1), l4); PIN(l4); SBAR(); \
            p0[2] = cvtpk(SC[4], SC[5]); p0[3] = cvtpk(SC[6], SC[7]); PIN(p0); SBAR(); EN(SC, 8, 2); PIN(SC); SBAR(); \
            o = MFMA32(vf0, PB(p0), o); PIN(o); SBAR(); \
            vf0 = ld128(lp + (v0n)); SBAR(); \
            EN(SC, 10, 3); PIN(SC); SBAR(); \
            l4 = MFMA16(sel, PB(p0), l4); PIN(l4); SBAR(); \
            EN(SC, 13, 3); \
            _Pragma("unroll") for (int i = 0; i < 4; ++i) p1[i] = cvtpk(SC[8 + 2 * i], SC[9 + 2 * i]); \
            PIN(p1); SBAR(); \
        } while (0)
#define A_ITER(st) do { \
            constexpr int b0 = (st) * M_KS; \
            bar(); \
            A_STEP(sA, sB, b0 + 1024, b0, b0 + 3072); \
            A_STEP(sB, sA, b0 + 5120, b0 + 4096, b0 + 7168); \
            } while (0)
        static_assert(NA % 4 == 0 && NY % 4 == 0 && NO % 4 == 0, "slot phase");
        LGKM0();
        for (int it = 0; it < 8; ++it) { A_ITER(0); A_ITER(1); A_ITER(2); A_ITER(3); }
        bar();
        o = MFMA32(vf1, PB(p1), o); l4 = MFMA16(sel, PB(p1), l4);
        float l = ((lane >> 4) & 1) ? l4[1] : l4[0];
        bool ok = l < 1.0e30f;
#pragma unroll
        for (int i = 0; i < 16; ++i) ok = ok && (fabsf(o[i]) < 3.0e38f);
        if (!__all(ok)) {
            float m = -INFINITY, ls = 0.f; o = (f32x16){};
            const char* gp = kb + lane * 16;
            for (int t = 0; t < 64; ++t) {
                bf16x8 k0, k1, v0, v1;
                asm volatile("global_load_dwordx4 %0, %4, off sc1\n\tglobal_load_dwordx4 %1, %4, off offset:1024 sc1\n\t"
                             "global_load_dwordx4 %2, %5, off sc1\n\tglobal_load_dwordx4 %3, %5, off offset:1024 sc1\n\ts_waitcnt vmcnt(0)"
                             : "=&v"(k0), "=&v"(k1), "=&v"(v0), "=&v"(v1) : "v"(gp + t * 2048), "v"(gp + (4 << 20) + t * 2048) : "memory");
                f32x16 s = MFMA32(k0, qf[0], (f32x16){}); s = MFMA32(k1, qf[1], s);
                float tm = s[0];
#pragma unroll
                for (int i = 1; i < 16; ++i) tm = fmaxf(tm, s[i]);
                tm = fmaxf(tm, __shfl_xor(tm, 32));
                const float mn = fmaxf(m, tm), al = EX2(m - mn);
                m = mn; ls *= al;
#pragma unroll
                for (int i = 0; i < 16; ++i) { o[i] *= al; s[i] = EX2(s[i] - mn); ls += s[i]; }
                u32x4 a0, a1;
#pragma unroll
                for (int i = 0; i < 4; ++i) { a0[i] = cvtpk(s[2 * i], s[2 * i + 1]); a1[i] = cvtpk(s[8 + 2 * i], s[9 + 2 * i]); }
                o = MFMA32(v0, PB(a0), o); o = MFMA32(v1, PB(a1), o);
            }
            l = ls + __shfl_xor(ls, 32);
        }
        { const float sc = omg / l;
          char* op = ws + WS_O + ((size_t)qt * 64 + 2 * head) * 1024 + lane * 16;
#pragma unroll
          for (int s = 0; s < 2; ++s) ST16(op + s * 1024, pack8(o, s, sc)); }
        bars(2);
        drain();
        bar();
        if (w == 0) {
            if (lane == 0) st_tag(myflag + 4096, tag);
            const int hd = lane & 31;
            const char* fpn = ws + WS_F + 4096 + (((hd & 3) * 8 + (tt >> 1)) * 8 + (hd >> 2)) * 8;
            bars(4);
            const u64 v = ld_tag(fpn);
            bars(2);
            tags_acquire(fpn, v, tag);
        } else bars(6);
        bars(2);
        {
            const int wm = w >> 1, wn = w & 1;
            const int q4 = (lane & 15) >> 2, p4 = lane & 3, cg = (lane >> 4) & 1, pi4 = ((p4 & 1) << 1) | (p4 >> 1);
            lds_cptr ap[2], bp[2];
#pragma unroll
            for (int h = 0; h < 2; ++h) {
                ap[h] = lp + M_G0 + h * 2 * M_GS + wm * 4096;
                bp[h] = (lds_cptr)lds + M_G0 + h * 2 * M_GS + M_H + (8 * hh + q4) * 128 + ((wn ^ (q4 >> 1)) << 6) + cg * 32 + pi4 * 8;
            }
            f32x16 acc = (f32x16){};
            bf16x8 a0[4], b0[4], a1[4], b1[4];
#pragma unroll
            for (int i = 0; i < 4; ++i) { a1[i] = (bf16x8){}; b1[i] = (bf16x8){}; }
#define F_ITER(st, AP, BP, AC, BC) do { \
                bar0(); \
                _Pragma("unroll") for (int i = 0; i < 4; ++i) { \
                    AC[i] = ld128(ap[(st) >> 1] + ((st) & 1) * M_GS + i * 1024); BC[i] = ldtr(bp[(st) >> 1] + ((st) & 1) * M_GS + i * 2048); SBAR(); \
                    acc = MFMA32(BP[i], AP[i], acc); PIN(acc); SBAR(); } \
                } while (0)
            LGKM0();
            for (int it = 0; it < 4; ++it) { F_ITER(0, a1, b1, a0, b0); F_ITER(1, a0, b0, a1, b1); F_ITER(2, a1, b1, a0, b0); F_ITER(3, a0, b0, a1, b1); }
#pragma unroll
            for (int i = 0; i < 4; ++i) acc = MFMA32(b1[i], a1[i], acc);
#pragma unroll
            for (int s = 0; s < 2; ++s) {
                const u32x4 ya = *(const LAS u32x4*)(lp + w * 2048 + s * 1024), yb = *(const LAS u32x4*)(lp + 16384 + w * 2048 + s * 1024);
#pragma unroll
                for (int i = 0; i < 4; ++i) {
                    acc[8 * s + 2 * i] += __uint_as_float(ya[i] << 16) + __uint_as_float(yb[i] << 16);
                    acc[8 * s + 2 * i + 1] += __uint_as_float(ya[i] & 0xffff0000u) + __uint_as_float(yb[i] & 0xffff0000u);
                }
            }
            u16* op = out + (size_t)(tt * 128 + wm * 32 + r) * 1024 + (2 * et + part) * 64 + wn * 32 + hh * 8;
#pragma unroll
            for (int s = 0; s < 2; ++s) ST16(op + s * 16, pack8(acc, s, 1.f));
        }
        if (blockIdx.x == 0 && w == 0 && lane == 0) st_tag(ws + WS_E, tag);
    }
}

extern "C" void kernel_launch(void* const* d_in, const int* in_sizes, int n_in, void* d_out, int out_size, void* d_ws, size_t ws_size,
                              hipStream_t stream) {
    (void)hipFuncSetAttribute((const void*)k_all, hipFuncAttributeMaxDynamicSharedMemorySize, M_LDS);
    k_all<<<256, 1024, M_LDS, stream>>>((const u16*)d_in[0], (const u16*)d_in[1], (const u16*)d_in[2], (const u16*)d_in[3], (const u16*)d_in[4],
                                        (const u16*)d_in[6], (const u16*)d_in[7], (const u16*)d_in[8], (char*)d_ws, (u16*)d_out);
}
```

```cpp
#include <hip/hip_runtime.h>
#include <hip/hip_bf16.h>

#define WARM 0
#define RTSTAMP 0
typedef unsigned short u16;
typedef short bf16x8 __attribute__((ext_vector_type(8)));
typedef short s16x4 __attribute__((ext_vector_type(4)));
typedef float f32x16 __attribute__((ext_vector_type(16)));
typedef float f32x4 __attribute__((ext_vector_type(4)));
typedef float f32x2 __attribute__((ext_vector_type(2)));
typedef unsigned u32x4 __attribute__((ext_vector_type(4)));
#define LAS __attribute__((address_space(3)))
typedef LAS const char* lds_cptr;

constexpr size_t WS_Q = 0;
constexpr size_t WS_KV = 4u << 20;
constexpr size_t WS_AB = 12u << 20;
constexpr size_t WS_O = 20u << 20;
constexpr size_t WS_Y = 24u << 20;
constexpr size_t WS_G = 40u << 20;
constexpr size_t WS_F = WS_G + 65536;
constexpr size_t WS_E = WS_F + 16384;

#define DIAG_BEGIN()
#define DIAG_MARK(i)
#define DIAG_END(kid, wsp)

#define MFMA32(a, b, c) __builtin_amdgcn_mfma_f32_32x32x16_bf16((a), (b), (c), 0, 0, 0)
#define MFMA16(a, b, c) __builtin_amdgcn_mfma_f32_16x16x32_bf16((a), (b), (c), 0, 0, 0)
#define SBAR() __builtin_amdgcn_sched_barrier(0)
#define PIN(x) asm volatile("" : "+v"(x))
#define EX2(v) __builtin_amdgcn_exp2f(v)
#define PB(p) __builtin_bit_cast(bf16x8, p)
#define LGKM0() __builtin_amdgcn_s_waitcnt(0xC07F)

__device__ __forceinline__ unsigned cvtpk(float lo, float hi) {
    typedef __bf16 bf16x2_t __attribute__((ext_vector_type(2)));
    return __builtin_bit_cast(unsigned, __builtin_convertvector((f32x2){lo, hi}, bf16x2_t)); }
__device__ __forceinline__ float bfr(float f) { return __uint_as_float(cvtpk(f, 0.f) << 16); }

__device__ __forceinline__ void glds16(unsigned voff, const void* sbase, unsigned ldsdst) {
    asm volatile("s_mov_b32 m0, %2\n\ts_nop 0\n\tglobal_load_lds_dwordx4 %0, %1" :: "v"(voff), "s"(sbase), "s"(ldsdst) : "memory");
}
#define DSC " sc1"
__device__ __forceinline__ void glds16d(unsigned voff, const void* sbase, unsigned ldsdst) {
    asm volatile("s_mov_b32 m0, %2\n\ts_nop 0\n\tglobal_load_lds_dwordx4 %0, %1" DSC :: "v"(voff), "s"(sbase), "s"(ldsdst) : "memory");
}
__device__ __forceinline__ void glds2d(unsigned v0, unsigned v1, const void* sbase, unsigned ldsdst) {
    asm volatile("s_mov_b32 m0, %3\n\ts_nop 0\n\tglobal_load_lds_dwordx4 %0, %2" DSC "\n\tglobal_load_lds_dwordx4 %1, %2 offset:1024" DSC
                 :: "v"(v0), "v"(v1), "s"(sbase), "s"(ldsdst) : "memory");
}
__device__ __forceinline__ void glds4d(unsigned v0, unsigned v1, unsigned v2, unsigned v3, const void* sbase, unsigned ldsdst) {
    asm volatile("s_mov_b32 m0, %5\n\ts_nop 0\n\tglobal_load_lds_dwordx4 %0, %4" DSC "\n\tglobal_load_lds_dwordx4 %1, %4 offset:1024" DSC "\n\t"
                 "global_load_lds_dwordx4 %2, %4 offset:2048" DSC "\n\tglobal_load_lds_dwordx4 %3, %4 offset:3072" DSC
                 :: "v"(v0), "v"(v1), "v"(v2), "v"(v3), "s"(sbase), "s"(ldsdst) : "memory");
}
__device__ __forceinline__ void glds2(unsigned v0, unsigned v1, const void* sbase, unsigned ldsdst) {
    asm volatile("s_mov_b32 m0, %3\n\ts_nop 0\n\tglobal_load_lds_dwordx4 %0, %2\n\tglobal_load_lds_dwordx4 %1, %2 offset:1024"
                 :: "v"(v0), "v"(v1), "s"(sbase), "s"(ldsdst) : "memory");
}
__device__ __forceinline__ void glds4(unsigned v0, unsigned v1, unsigned v2, unsigned v3, const void* sbase, unsigned ldsdst) {
    asm volatile("s_mov_b32 m0, %5\n\ts_nop 0\n\tglobal_load_lds_dwordx4 %0, %4\n\tglobal_load_lds_dwordx4 %1, %4 offset:1024\n\t"
                 "global_load_lds_dwordx4 %2, %4 offset:2048\n\tglobal_load_lds_dwordx4 %3, %4 offset:3072"
                 :: "v"(v0), "v"(v1), "v"(v2), "v"(v3), "s"(sbase), "s"(ldsdst) : "memory");
}
template <int N> __device__ __forceinline__ void wait_bar() {
    SBAR();
    asm volatile("s_waitcnt vmcnt(%0)\n\ts_barrier" :: "n"(N) : "memory");
    SBAR();
}
__device__ __forceinline__ void bar() { SBAR(); asm volatile("s_barrier" ::: "memory"); SBAR(); }
__device__ __forceinline__ void bar0() { SBAR(); asm volatile("s_waitcnt lgkmcnt(0)\n\ts_barrier" ::: "memory"); SBAR(); }
__device__ __forceinline__ void bar_l1() {
    SBAR();
    asm volatile("s_waitcnt lgkmcnt(1)\n\ts_barrier" ::: "memory");
    SBAR();
}
__device__ __forceinline__ bf16x8 ld128(lds_cptr p) { return *(const LAS bf16x8*)p; }
__device__ __forceinline__ bf16x8 ldtr(lds_cptr p) {
    s16x4 lo = __builtin_amdgcn_ds_read_tr16_b64_v4i16((LAS s16x4*)p);
    s16x4 hi = __builtin_amdgcn_ds_read_tr16_b64_v4i16((LAS s16x4*)(p + 512));
    return (bf16x8){lo[0], lo[1], lo[2], lo[3], hi[0], hi[1], hi[2], hi[3]};
}
__device__ __forceinline__ u32x4 pack8(const f32x16& a, int s, float sc) {
    u32x4 w;
    w[0] = cvtpk(a[8 * s + 0] * sc, a[8 * s + 1] * sc); w[1] = cvtpk(a[8 * s + 2] * sc, a[8 * s + 3] * sc);
    w[2] = cvtpk(a[8 * s + 4] * sc, a[8 * s + 5] * sc); w[3] = cvtpk(a[8 * s + 6] * sc, a[8 * s + 7] * sc);
    return w;
}
#define STF " sc1"
#define ST16(p, v) do { const u32x4 v_ = (v); asm volatile("global_store_dwordx4 %0, %1, off" STF "\n\ts_nop 1" :: "v"(p), "v"(v_) : "memory"); } while (0)
__device__ __forceinline__ int swap23(int i) { return (i & ~12) | ((i & 4) << 1) | ((i & 8) >> 1); }

__device__ __forceinline__ int lf_order(int tau, int a, int c, int G) {
    if (tau == 0) return a;
    const int g = (tau - 1) >> 2, pos = (tau - 1) & 3;
    if (g != G - 1 && pos == c) return 4 * (g + 1) + a;
    const int b = (g == G - 1 || pos < c) ? pos : pos - 1;
    return 4 * g + (b < a ? b : b + 1);
}
#define NX3(v) ((v) == 2 ? 0 : (v) + 1)
#define NX5(v) ((v) == 4 ? 0 : (v) + 1)

__device__ __forceinline__ int rswz(int row) { return ((row >> 1) & 1) | (((row >> 3) & 3) << 1); }
__device__ __forceinline__ u32x4 pack44(const f32x4& a, const f32x4& b, float sc) {
    return (u32x4){cvtpk(a[0] * sc, a[1] * sc), cvtpk(a[2] * sc, a[3] * sc), cvtpk(b[0] * sc, b[1] * sc), cvtpk(b[2] * sc, b[3] * sc)};
}
__device__ __forceinline__ void drain() { asm volatile("s_waitcnt vmcnt(0)" ::: "memory"); }
typedef unsigned long long u64;
__device__ __forceinline__ u64 ld_tag(const char* p) { return __hip_atomic_load((const u64*)p, __ATOMIC_RELAXED, __HIP_MEMORY_SCOPE_AGENT); }
__device__ __forceinline__ void st_tag(char* p, u64 v) { __hip_atomic_store((u64*)p, v, __ATOMIC_RELAXED, __HIP_MEMORY_SCOPE_AGENT); }
__device__ __forceinline__ void tags_acquire(const char* p, u64 v, u64 tag) {
#pragma nounroll
    for (int spin = 0; !__all(v == tag) && spin < (1 << 22); ++spin) { __builtin_amdgcn_s_sleep(2); v = ld_tag(p); }
    asm volatile("" ::: "memory");
}
__device__ __forceinline__ u64 call_tag(const char* ws) {
    const u64 e = ld_tag(ws + WS_E) + 1;
    return ((u64)(unsigned)__builtin_amdgcn_readfirstlane((int)(e >> 32)) << 32) | (unsigned)__builtin_amdgcn_readfirstlane((int)e);
}
__device__ __forceinline__ int fresh_lane() {
    int l; asm volatile("v_mbcnt_lo_u32_b32 %0, -1, 0\n\tv_mbcnt_hi_u32_b32 %0, -1, %0" : "=v"(l)); return l;
}
__device__ __forceinline__ float DOT2(unsigned a, unsigned b, float acc) {
    typedef __bf16 bf2_t __attribute__((ext_vector_type(2)));
    return __builtin_amdgcn_fdot2_f32_bf16(__builtin_bit_cast(bf2_t, a), __builtin_bit_cast(bf2_t, b), acc, false);
}
__device__ __forceinline__ void bars(int n) { for (int i = 0; i < n; ++i) bar(); }

constexpr int M_KS = 8192, M_G0 = 4 * M_KS, M_GS = 32768, M_H = 16384, M_LDS = M_G0 + 4 * M_GS;
constexpr int CODE_KB = 23;
constexpr int NA = 32, NY = 44, NO = 76;
__global__ __launch_bounds__(1024, 4) void k_all(const u16* __restrict__ x, const u16* __restrict__ cr, const u16* __restrict__ ci,
                                                  const u16* __restrict__ hr, const u16* __restrict__ hi, const u16* __restrict__ wqkv,
                                                  const u16* __restrict__ wo, const u16* __restrict__ gw, char* ws, u16* __restrict__ out) {
    extern __shared__ __attribute__((aligned(16))) char smem[];
    LAS char* lds = (LAS char*)smem;
    unsigned long long pc0; asm volatile("s_getpc_b64 %0" : "=s"(pc0));
    DIAG_BEGIN();
    const int w = (__builtin_amdgcn_readfirstlane(threadIdx.x >> 6) + 12) & 15;
    const int xcd = blockIdx.x & 7, idx = blockIdx.x >> 3;
    const int rt = (xcd & 1) * 8 + (idx & 7), j = (xcd >> 1) * 4 + (idx >> 3);
    const int head = xcd * 4 + (idx >> 3), qb = idx & 7;
    const int part = xcd >> 2, tt = ((xcd >> 1) & 1) * 8 + (idx & 7), et = (xcd & 1) * 4 + (idx >> 3);
    const char* kb = ws + WS_KV + (size_t)head * 131072;
#define LANE_VARS() const int lane = fresh_lane(), r = lane & 31, hh = lane >> 5, c = lane & 15, g4 = lane >> 4, sg0 = 8 * (c >> 2) + (c & 3); \
    lds_cptr lp = (lds_cptr)lds + lane * 16; (void)r; (void)hh; (void)g4; (void)sg0; (void)lp
    char* myflag = ws + WS_F + blockIdx.x * 8;
    if (w >= 12) {
        LANE_VARS();
        const int g = w - 12;
        const int la = ((idx >> 3) - (idx & 7)) & 3, lc = (idx >> 1) & 3;
        const int tab16 = lf_order(lane & 15, la, lc, 4), tab32 = lf_order(lane & 31, la, lc, 8);
        const unsigned lds0 = (unsigned)(uintptr_t)lds;
        const unsigned vL = (unsigned)(lane * 16);
        unsigned vR[4], vW[2], vC[4], vA[2], vT[2];
#pragma unroll
        for (int i = 0; i < 4; ++i) {
            vR[i] = (unsigned)((8 * i + (lane >> 3)) * 2048 + (((lane & 7) ^ rswz(8 * i + (lane >> 3))) << 4) - i * 1024);
            vC[i] = (unsigned)((8 * i + (lane >> 3)) * 4096 + (((lane & 7) ^ ((4 * i + (lane >> 4)) & 7)) << 4) - i * 1024);
        }
#pragma unroll
        for (int i = 0; i < 2; ++i) {
            vW[i] = (unsigned)((8 * i + (lane >> 3)) * 6144 + (((lane & 7) ^ (((lane >> 4) & 1) << 2) ^ (i << 1)) << 4) - i * 1024);
            vT[i] = (unsigned)((8 * i + (lane >> 3)) * 2048 + (((lane & 7) ^ (((lane >> 4) & 1) << 2)) << 4) - i * 1024);
            vA[i] = (unsigned)(lane * 16 + i * (65536 - 2048));
        }
        const unsigned vKV = (unsigned)(lane * 16 + (4 << 20) - 3072 - 1024);
        const char* xb = (const char*)x + (size_t)(rt * 128 + g * 32) * 2048;
        const char* hb = (const char*)(j < 8 ? hr : hi) + (size_t)((j & 7) * 128 + g * 32) * 2048;
        const char* wb = (const char*)wqkv + (size_t)g * 16 * 6144 + j * 128;
        const char* cb = (const char*)(part ? ci : cr) + (size_t)(tt * 128 + 32 * g) * 4096;
        const char* ab = ws + WS_AB + (size_t)(part * 64 + et * 8 + 2 * g) * 65536;
        const char* ob = ws + WS_O + (size_t)(tt * 4 + g) * 65536;
        const char* wob = (const char*)wo + (2 * et + part) * 128;
        const unsigned dG = lds0 + M_G0 + g * 4096, dK = lds0 + g * 2048;
        auto sP = [&](int n) {
            const int kp = __builtin_amdgcn_readlane(tab16, n); const unsigned so = (n & 3) * M_GS;
            const char* b = wb + (size_t)(kp * (64 * 6144));
            glds4(vR[0], vR[1], vR[2], vR[3], xb + kp * 128, dG + so);
            glds2(vW[0], vW[1], b, dG + so + M_H - g * 2048); glds2(vW[0], vW[1], b + 2048, dG + so + M_H + 8192 - g * 2048);
            glds2(vW[0], vW[1], b + 4096, dK + (n & 3) * M_KS);
        };
        auto sH = [&](int n) {
            const int kp = __builtin_amdgcn_readlane(tab16, n & 15); const unsigned so = (n & 3) * M_GS;
            glds4(vR[0], vR[1], vR[2], vR[3], xb + kp * 128, dG + so); glds4(vR[0], vR[1], vR[2], vR[3], hb + kp * 128, dG + so + M_H);
        };
        auto sKV = [&](int n) {
            glds2d(vL, vKV, kb + ((n - NA) * 4096 + 4096 + g * 1024), dK + (n & 3) * M_KS);
        };
        auto sY = [&](int n) {
            const int kp = __builtin_amdgcn_readlane(tab32, n - NY); const unsigned so = (n & 3) * M_GS;
            glds4(vC[0], vC[1], vC[2], vC[3], cb + kp * 128, dG + so); glds4d(vA[0], vA[0], vA[1], vA[1], ab + kp * 2048, dG + so + M_H);
        };
        auto sO = [&](int n) {
            const int kp = __builtin_amdgcn_readlane(tab16, n - NO); const unsigned so = (n & 3) * M_GS;
            glds2(vT[0], vT[1], wob + (size_t)((kp * 64 + g * 16) * 2048), dG + so + M_H - g * 2048);
            glds4d(vL, vL, vL, vL, ob + kp * 4096, dG + so);
        };
#define STAMP()
#define STAMP_DUMP()
#define WB(N) do { wait_bar<N>(); STAMP(); } while (0)
        auto sC = [&](int n) { const int kp = __builtin_amdgcn_readlane(tab32, n - NY); glds4(vC[0], vC[1], vC[2], vC[3], cb + kp * 128, dG + (n & 3) * M_GS); };
        auto sAB = [&](int n) { const int kp = __builtin_amdgcn_readlane(tab32, n - NY); glds4d(vA[0], vA[0], vA[1], vA[1], ab + kp * 2048, dG + (n & 3) * M_GS + M_H); };
        sP(0); sP(1); WB(10);
        sP(2);
        for (int k = 1; k < 14; ++k) { sP(k + 2); WB(20); }
        sH(16); WB(18);
        for (int k = 15; k < 30; ++k) { sH(k + 2); WB(16); }
        glds16d(vL, kb + g * 1024, dK + 3 * M_KS); glds16d(vL, kb + (4 << 20), dK + 3 * M_KS + 1024);
        WB(10);
        u32x4 wr[12][2];
#define LDW(i) do { const int kp_ = __builtin_amdgcn_readlane(tab16, i); const char* b_ = wob + (size_t)((kp_ * 64 + g * 16) * 2048); \
            asm volatile("global_load_dwordx4 %0, %2, %4\n\tglobal_load_dwordx4 %1, %3, %4 offset:1024" \
                         : "=&v"(wr[i][0]), "=&v"(wr[i][1]) : "v"(vT[0]), "v"(vT[1]), "s"(b_) : "memory"); } while (0)
        sKV(32); WB(2);
        sKV(33); LDW(0); LDW(1); WB(6);
        sKV(34); LDW(2); LDW(3); WB(10);
        sKV(35); LDW(4); LDW(5); WB(10);
        sKV(36); LDW(6); LDW(7); WB(10);
        sKV(37); sC(44); WB(10);
        for (int k = 37; k < 40; ++k) { sKV(k + 1); sC(k + 8); WB(10); }
        sKV(41); LDW(8); LDW(9); WB(10);
        sKV(42); LDW(10); LDW(11); WB(10);
        sKV(43); WB(6);
        sKV(44); sAB(44); sAB(45); WB(10);
        sKV(45); sAB(46); WB(10);
        sKV(46); sAB(47); WB(10);
        sKV(47); sY(48); WB(14);
        for (int k = 47; k < 62; ++k) { sKV(k + 1); sY(k + 2); WB(18); }
        { const int vo = g < 3 ? 31 * 4096 + 1024 + g * 1024 : 0;
          glds16d(vL, kb + (4 << 20) + vo, dK + 3 * M_KS + 1024); sY(64); WB(17); }
        for (int k = 63; k < 70; ++k) { sY(k + 2); WB(16); }
        glds16d(vL, ws + WS_G + (size_t)tt * 1024, lds0 + 16384);
        sY(72); WB(17);
        sY(73); WB(17);
        sY(74); WB(16);
        sY(75); WB(16);
        WB(8);
        LAS char* wdst = lds + M_G0 + M_H + g * 2048 + lane * 16;
#define SOR(i) do { const int kp_ = __builtin_amdgcn_readlane(tab16, i); glds4d(vL, vL, vL, vL, ob + kp_ * 4096, dG + ((i) & 3) * M_GS); \
            asm volatile("" : "+v"(wr[i][0]), "+v"(wr[i][1])); \
            *(LAS u32x4*)(wdst + ((i) & 3) * M_GS) = wr[i][0]; *(LAS u32x4*)(wdst + ((i) & 3) * M_GS + 1024) = wr[i][1]; \
            asm volatile("s_waitcnt lgkmcnt(0)" ::: "memory"); } while (0)
        SOR(0); SOR(1); WB(8);
        SOR(2); WB(8); SOR(3); WB(8); SOR(4); WB(8); SOR(5); WB(8); SOR(6); WB(8); SOR(7); WB(8); SOR(8); WB(8); SOR(9); WB(8);
        SOR(10); WB(8); SOR(11); WB(8);
        sO(88); WB(10);
        sO(89); WB(12);
        sO(90); WB(12);
        sO(91); glds4d(vL, vL, vL, vL, ws + WS_Y + (size_t)(blockIdx.x ^ 4) * 16384 + g * 4096, lds0 + 16384 + g * 4096);
        WB(16);
        WB(10);
        WB(0);
        static_assert(NA == 32 && NY == 44 && NO == 76, "the DMA program is written for this schedule");
        STAMP_DUMP();
    } else if (w >= 8) {
        LANE_VARS();
        const int g = w - 8, gm = g >> 1, gn = g & 1;
        { const int piece = (idx & 7) * 4 + g;
          if (piece < CODE_KB) { u32x4 v; asm volatile("global_load_dwordx4 %0, %1, %2\n\ts_waitcnt vmcnt(0)" : "=&v"(v) : "v"(piece * 1024 + lane * 16), "s"(pc0 & ~255ull) : "memory"); } }
        const u64 tag = call_tag(ws);
        __builtin_amdgcn_s_setprio(3);
        bars(2);
        {
            const u32x4* xr = (const u32x4*)(x + (size_t)(blockIdx.x * 8 + 2 * g) * 1024); const u32x4* gr = (const u32x4*)gw;
            const u32x4 a0 = xr[lane], a1 = xr[lane + 64], a2 = xr[lane + 128], a3 = xr[lane + 192], b0 = gr[lane], b1 = gr[lane + 64];
            bars(4);
            float z0 = 0.f, z1 = 0.f;
#pragma unroll
            for (int e = 0; e < 4; ++e) { z0 = DOT2(a0[e], b0[e], z0); z1 = DOT2(a2[e], b0[e], z1); z0 = DOT2(a1[e], b1[e], z0); z1 = DOT2(a3[e], b1[e], z1); }
#define DPP_ADD(v, ctrl, rmask) v += __uint_as_float(__builtin_amdgcn_update_dpp(0u, __float_as_uint(v), ctrl, rmask, 0xf, false))
            DPP_ADD(z0, 0x111, 0xf); DPP_ADD(z1, 0x111, 0xf); DPP_ADD(z0, 0x112, 0xf); DPP_ADD(z1, 0x112, 0xf);
            DPP_ADD(z0, 0x114, 0xf); DPP_ADD(z1, 0x114, 0xf); DPP_ADD(z0, 0x118, 0xf); DPP_ADD(z1, 0x118, 0xf);
            DPP_ADD(z0, 0x142, 0xa); DPP_ADD(z1, 0x142, 0xa); DPP_ADD(z0, 0x143, 0xc); DPP_ADD(z1, 0x143, 0xc);
            z0 = __uint_as_float(__builtin_amdgcn_readlane(__float_as_uint(z0), 63)); z1 = __uint_as_float(__builtin_amdgcn_readlane(__float_as_uint(z1), 63));
            bar();
            const float gt = bfr(1.f / (1.f + __expf(-bfr((lane & 1) ? z1 : z0))));
            if (lane < 2) st_tag(ws + WS_G + (size_t)(blockIdx.x * 8 + 2 * g + lane) * 8, ((u64)__float_as_uint(bfr(1.f - gt)) << 32) | __float_as_uint(gt));
            bars(6);
            __builtin_amdgcn_s_setprio(0);
            bars(2);
        }
        f32x4 y[4][4];
        bf16x8 cA[4], cB[4], fa[4];
        lds_cptr ca[2][2], fp[2][2];
#define G_MM(SW, a_, CX, m_) y[a_][m_] = (SW) ? MFMA16(CX[m_], fa[a_], y[a_][m_]) : MFMA16(fa[a_], CX[m_], y[a_][m_])
#define G_STEP(SW, CP, CC, ST, HF, CO, FO) do { \
            _Pragma("unroll") for (int s_ = 0; s_ < 4; ++s_) { \
                _Pragma("unroll") for (int m = 0; m < 4; ++m) { \
                    if (s_ < 2) G_MM(SW, s_ + 2, CP, m); else G_MM(SW, s_ - 2, CC, m); \
                    if (s_ == 0) CC[m] = ld128(ca[(ST) >> 1][HF] + ((ST) & 1) * M_GS + CO(m)); \
                    if (m == 0 && s_ < 3) fa[s_] = ld128(fp[(ST) >> 1][HF] + ((ST) & 1) * M_GS + FO(s_)); \
                    if (m == 2 && s_ == 2) fa[3] = ld128(fp[(ST) >> 1][HF] + ((ST) & 1) * M_GS + FO(3)); \
                    SBAR(); } } } while (0)
#define G_ITER(SW, st, CO, FO) do { bar0(); G_STEP(SW, cB, cA, st, 0, CO, FO); G_STEP(SW, cA, cB, st, 1, CO, FO); } while (0)
#define G_RUN(SW, NIT, CO, FO, PRE) do { \
            _Pragma("unroll") for (int a = 0; a < 4; ++a) { cB[a] = (bf16x8){}; fa[a] = (bf16x8){}; \
                _Pragma("unroll") for (int b = 0; b < 4; ++b) y[a][b] = (f32x4){}; } \
            PRE; LGKM0(); \
            for (int it = 0; it < NIT; ++it) { G_ITER(SW, 0, CO, FO); G_ITER(SW, 1, CO, FO); G_ITER(SW, 2, CO, FO); G_ITER(SW, 3, CO, FO); } \
            bar0(); \
            _Pragma("unroll") for (int a = 2; a < 4; ++a) _Pragma("unroll") for (int m = 0; m < 4; ++m) G_MM(SW, a, cB, m); } while (0)
#define OFF_SIG(i) (((i) >> 1) * 4096 + ((i) & 1) * 512)
#define OFF_2K(i) ((i) * 2048)
#pragma unroll
        for (int h = 0; h < 2; ++h)
#pragma unroll
            for (int hf = 0; hf < 2; ++hf) {
                const int ra = M_G0 + h * 2 * M_GS + sg0 * 128 + (((4 * hf + g4) ^ rswz(sg0)) << 4);
                ca[h][hf] = (lds_cptr)lds + ra + gm * 8192; fp[h][hf] = (lds_cptr)lds + ra + M_H + gn * 8192; PIN(ca[h][hf]); PIN(fp[h][hf]);
            }
        G_RUN(true, 4, OFF_SIG, OFF_SIG, drain(); bar());
      { LANE_VARS();
#pragma unroll
        for (int gr = 0; gr < 2; ++gr) {
            char* abp = ws + WS_AB + ((size_t)(j * 8 + gn * 4) * 64 + rt * 4 + gm * 2 + gr) * 1024 + lane * 16;
#pragma unroll
            for (int b = 0; b < 4; ++b) ST16(abp + (size_t)b * 65536, pack44(y[b][2 * gr], y[b][2 * gr + 1], 1.f));
            bar();
        }
        bar();
        drain();
        bar();
        if (g == 0) {
            if (lane == 0) st_tag(myflag + 2048, tag);
            const int jj = part * 8 + et, rr = lane & 15;
            const char* fpn = ws + WS_F + 2048 + (((rr & 7) | ((jj & 3) << 3)) * 8 + ((rr >> 3) | ((jj >> 2) << 1))) * 8;
            bars(4);
            const u64 v = ld_tag(fpn);
            bar();
            tags_acquire(fpn, v, tag);
        } else bars(5);
        bar();
#pragma unroll
        for (int h = 0; h < 2; ++h)
#pragma unroll
            for (int hf = 0; hf < 2; ++hf) {
                ca[h][hf] = (lds_cptr)lds + M_G0 + h * 2 * M_GS + (gm * 64 + c) * 128 + (((4 * hf + g4) ^ ((c >> 1) & 7)) << 4);
                fp[h][hf] = lp + M_G0 + h * 2 * M_GS + M_H + gn * 8192 + hf * 1024; PIN(ca[h][hf]); PIN(fp[h][hf]);
            }
      }
        G_RUN(false, 8, OFF_2K, OFF_2K, bar());
      { LANE_VARS();
        __builtin_amdgcn_s_setprio(3);
#pragma unroll
        for (int m = 0; m < 4; ++m) {
            float gy = *(const LAS float*)(lds + 16384 + (gm * 64 + m * 16 + c) * 8);
            if (part) gy = -gy;
#pragma unroll
            for (int e = 0; e < 2; ++e) {
                const int off = ((gm * 2 + (m >> 1)) * 2 + e) * 2048 + (g4 >> 1) * 1024 + ((g4 & 1) * 32 + (m & 1) * 16 + c) * 16;
                const u32x4 v = pack44(y[2 * e][m], y[2 * e + 1][m], gy);
                if (gn == part) *(LAS u32x4*)(lds + off) = v; else ST16(ws + WS_Y + (size_t)blockIdx.x * 16384 + off, v);
            }
            bar();
        }
        bars(2);
        asm volatile("s_waitcnt vmcnt(0) lgkmcnt(0)" ::: "memory");
        bar();
        if (g == 0) {
            if (lane == 0) st_tag(myflag + 6144, tag);
            const char* fpn = ws + WS_F + 6144 + (blockIdx.x ^ 4) * 8;
            bars(3);
            const u64 v = ld_tag(fpn);
            bar();
            tags_acquire(fpn, v, tag);
            bar();
        }
      }
    } else {
        LANE_VARS();
        const u64 tag = call_tag(ws);
        {
            const int wm = w >> 2, wn = (w >> 1) & 1, wq = w & 1;
            lds_cptr xa[2][2], tq[2][2], tv;
            { const int q4 = c >> 2, p4 = c & 3;
#pragma unroll
              for (int i = 0; i < 2; ++i) {
                  const int tbo = (8 * g4 + q4) * 128 + ((((wn * 4 + i * 2) ^ ((q4 >> 1) << 2) ^ ((g4 & 1) << 1)) + (p4 >> 1)) << 4) + (p4 & 1) * 8;
#pragma unroll
                  for (int h = 0; h < 2; ++h) {
                      xa[h][i] = (lds_cptr)lds + M_G0 + h * 2 * M_GS + wm * 8192 + sg0 * 128 + (((4 * i + g4) ^ rswz(sg0)) << 4); PIN(xa[h][i]);
                      tq[h][i] = (lds_cptr)lds + M_G0 + h * 2 * M_GS + M_H + wq * 8192 + tbo; PIN(tq[h][i]);
                  } }
              tv = (lds_cptr)lds + (8 * g4 + q4) * 128 + ((((wn * 4 + wq * 2) ^ ((q4 >> 1) << 2) ^ ((g4 & 1) << 1)) + (p4 >> 1)) << 4) + (p4 & 1) * 8; PIN(tv); }
            f32x4 acc[3][4];
            bf16x8 xA[4], xB[4], fb[3];
#pragma unroll
            for (int m = 0; m < 4; ++m) { acc[0][m] = (f32x4){}; acc[1][m] = (f32x4){}; acc[2][m] = (f32x4){}; xB[m] = (bf16x8){}; }
            fb[1] = fb[2] = (bf16x8){};
#define Q_MM(b_, XF, m_) acc[b_][m_] = (b_) < 2 ? MFMA16(fb[b_], XF[m_], acc[b_][m_]) : MFMA16(XF[m_], fb[b_], acc[b_][m_])
#define Q_STEP(XP, XC, ST, HF) do { \
                _Pragma("unroll") for (int s_ = 0; s_ < 3; ++s_) { \
                    _Pragma("unroll") for (int m = 0; m < 4; ++m) { \
                        if (s_ < 2) Q_MM(s_ + 1, XP, m); else Q_MM(0, XC, m); \
                        SBAR(); \
                        if (s_ < 2 && m >= 2) XC[2 * s_ + m - 2] = ld128(xa[(ST) >> 1][HF] + ((ST) & 1) * M_GS + s_ * 4096 + (m - 2) * 512); \
                        if (m == 0) fb[s_] = ldtr((HF) * 4096 + (s_ < 2 ? tq[(ST) >> 1][s_ & 1] + ((ST) & 1) * M_GS : tv + (ST) * M_KS)); \
                        SBAR(); } } } while (0)
#define Q_ITER(st) do { bar0(); Q_STEP(xB, xA, st, 0); Q_STEP(xA, xB, st, 1); } while (0)
            LGKM0();
            for (int it = 0; it < 4; ++it) { Q_ITER(0); Q_ITER(1); Q_ITER(2); Q_ITER(3); }
            bar0();
#pragma unroll
            for (int b = 1; b < 3; ++b)
#pragma unroll
                for (int m = 0; m < 4; ++m) Q_MM(b, xB, m);
            DIAG_MARK(0);
            const int hd = 2 * j + wn;
            const float qs = wq ? 1.f : 0.17677669529663687f * 1.4426950408889634f;
            char* bp = ws + (size_t)(hd * 64 + rt * 4 + wm * 2) * 2048 + (g4 >> 1) * 1024 + (g4 & 1) * 512;
            char* qk = bp + (wq ? WS_KV + (16 * (c >> 3) + (c & 7)) * 16 : WS_Q + sg0 * 16);
            const int as = wq ? 128 : 64;
#pragma unroll
            for (int gr = 0; gr < 2; ++gr) { ST16(bp + WS_KV + (4u << 20) + gr * 2048 + (16 * wq + swap23(c)) * 16, pack44(acc[2][2 * gr], acc[2][2 * gr + 1], 1.f)); }
            bar();
#pragma unroll
            for (int a = 0; a < 2; ++a) ST16(qk + a * as, pack44(acc[0][a], acc[1][a], qs));
            bar();
#pragma unroll
            for (int a = 0; a < 2; ++a) ST16(qk + 2048 + a * as, pack44(acc[0][2 + a], acc[1][2 + a], qs));
        }
        bars(3);
        drain();
        bar();
        if (w == 0) {
            if (lane == 0) st_tag(myflag, tag);
            const int jj = head >> 1, rr = lane & 15;
            const int pb = lane < 16 ? ((rr & 7) | ((jj & 3) << 3)) * 8 + ((rr >> 3) | ((jj >> 2) << 1)) : (lane < 32 ? tt * 16 + rr : qb * 32 + (lane - 32));
            const char* fpn = ws + WS_F + pb * 8;
            bars(4);
            const u64 v = ld_tag(fpn);
            bars(2);
            tags_acquire(fpn, v, tag);
        } else bars(6);
        bar();
        const int qt = qb * 8 + w;
        bf16x8 qf[2]; float omg;
        { const char* qp = ws + WS_Q + (size_t)(head * 64 + qt) * 2048 + lane * 16;
          asm volatile("global_load_dwordx4 %0, %1, off sc1" : "=v"(qf[0]) : "v"(qp) : "memory");
          asm volatile("global_load_dwordx4 %0, %1, off offset:1024 sc1" : "=v"(qf[1]) : "v"(qp) : "memory");
          asm volatile("global_load_dword %0, %1, off offset:4 sc1" : "=v"(omg) : "v"(ws + WS_G + (size_t)(qt * 32 + r) * 8) : "memory"); }
        bf16x8 sel; { const short one = (((lane & 1) == ((lane >> 4) & 1))) ? (short)0x3f80 : (short)0;
#pragma unroll
            for (int i = 0; i < 8; ++i) sel[i] = one; }
        f32x16 o = (f32x16){}; f32x4 l4 = (f32x4){};
        f32x16 negm, sA, sB;
        bf16x8 kf[2], vf0, vf1;
        u32x4 p0, p1;
        bars(2);
        asm volatile("s_waitcnt vmcnt(0)" : "+v"(qf[0]), "+v"(qf[1]), "+v"(omg) :: "memory");
        DIAG_MARK(1);
        kf[0] = ld128(lp + 3 * M_KS); kf[1] = ld128(lp + 3 * M_KS + 2048);
        sA = MFMA32(kf[0], qf[0], (f32x16){}); sA = MFMA32(kf[1], qf[1], sA);
        { float m = sA[0];
#pragma unroll
          for (int i = 1; i < 16; ++i) m = fmaxf(m, sA[i]);
          m = fmaxf(m, __shfl_xor(m, 32));
#pragma unroll
          for (int i = 0; i < 16; ++i) { negm[i] = -m; sA[i] -= m; } }
        kf[0] = ld128(lp + 3 * M_KS + 4096); kf[1] = ld128(lp + 3 * M_KS + 6144);
        vf0 = ld128(lp + 3 * M_KS + 1024);
        vf1 = vf0; p1 = (u32x4){0u, 0u, 0u, 0u};
#define EN(S, i, n) do { _Pragma("unroll") for (int e_ = 0; e_ < n; ++e_) S[i + e_] = EX2(S[i + e_]); } while (0)
#define A_STEP(SC, SN, v1o, kn, v0n) do { \
            SN = MFMA32(kf[0], qf[0], negm); PIN(SN); SBAR(); \
            EN(SC, 0, 3); PIN(SC); SBAR(); \
            o = MFMA32(vf1, PB(p1), o); PIN(o); SBAR(); \
            EN(SC, 3, 3); PIN(SC); SBAR(); \
            SN = MFMA32(kf[1], qf[1], SN); PIN(SN); SBAR(); \
            vf1 = ld128(lp + (v1o)); kf[0] = ld128(lp + (kn)); kf[1] = ld128(lp + (kn) + 2048); SBAR(); \
            EN(SC, 6, 2); p0[0] = cvtpk(SC[0], SC[1]); p0[1] = cvtpk(SC[2], SC[3]); PIN(SC); PIN(p0); SBAR(); \
            l4 = MFMA16(sel, PB(p1), l4); PIN(l4); SBAR(); \
            p0[2] = cvtpk(SC[4], SC[5]); p0[3] = cvtpk(SC[6], SC[7]); PIN(p0); SBAR(); EN(SC, 8, 2); PIN(SC); SBAR(); \
            o = MFMA32(vf0, PB(p0), o); PIN(o); SBAR(); \
            vf0 = ld128(lp + (v0n)); SBAR(); \
            EN(SC, 10, 3); PIN(SC); SBAR(); \
            l4 = MFMA16(sel, PB(p0), l4); PIN(l4); SBAR(); \
            EN(SC, 13, 3); \
            _Pragma("unroll") for (int i = 0; i < 4; ++i) p1[i] = cvtpk(SC[8 + 2 * i], SC[9 + 2 * i]); \
            PIN(p1); SBAR(); \
        } while (0)
#define A_ITER(st) do { \
            constexpr int b0 = (st) * M_KS; \
            bar(); \
            A_STEP(sA, sB, b0 + 1024, b0, b0 + 3072); \
            A_STEP(sB, sA, b0 + 5120, b0 + 4096, b0 + 7168); \
            } while (0)
        static_assert(NA % 4 == 0 && NY % 4 == 0 && NO % 4 == 0, "slot phase");
        LGKM0();
        for (int it = 0; it < 8; ++it) { A_ITER(0); A_ITER(1); A_ITER(2); A_ITER(3); }
        bar();
        o = MFMA32(vf1, PB(p1), o); l4 = MFMA16(sel, PB(p1), l4);
        DIAG_MARK(2);
        float l = ((lane >> 4) & 1) ? l4[1] : l4[0];
        bool ok = l < 1.0e30f;
#pragma unroll
        for (int i = 0; i < 16; ++i) ok = ok && (fabsf(o[i]) < 3.0e38f);
        if (!__all(ok)) {
            float m = -INFINITY, ls = 0.f; o = (f32x16){};
            const char* gp = kb + lane * 16;
            for (int t = 0; t < 64; ++t) {
                bf16x8 k0, k1, v0, v1;
                asm volatile("global_load_dwordx4 %0, %4, off sc1\n\tglobal_load_dwordx4 %1, %4, off offset:1024 sc1\n\t"
                             "global_load_dwordx4 %2, %5, off sc1\n\tglobal_load_dwordx4 %3, %5, off offset:1024 sc1\n\ts_waitcnt vmcnt(0)"
                             : "=&v"(k0), "=&v"(k1), "=&v"(v0), "=&v"(v1) : "v"(gp + t * 2048), "v"(gp + (4 << 20) + t * 2048) : "memory");
                f32x16 s = MFMA32(k0, qf[0], (f32x16){}); s = MFMA32(k1, qf[1], s);
                float tm = s[0];
#pragma unroll
                for (int i = 1; i < 16; ++i) tm = fmaxf(tm, s[i]);
                tm = fmaxf(tm, __shfl_xor(tm, 32));
                const float mn = fmaxf(m, tm), al = EX2(m - mn);
                m = mn; ls *= al;
#pragma unroll
                for (int i = 0; i < 16; ++i) { o[i] *= al; s[i] = EX2(s[i] - mn); ls += s[i]; }
                u32x4 a0, a1;
#pragma unroll
                for (int i = 0; i < 4; ++i) { a0[i] = cvtpk(s[2 * i], s[2 * i + 1]); a1[i] = cvtpk(s[8 + 2 * i], s[9 + 2 * i]); }
                o = MFMA32(v0, PB(a0), o); o = MFMA32(v1, PB(a1), o);
            }
            l = ls + __shfl_xor(ls, 32);
        }
        { const float sc = omg / l;
          char* op = ws + WS_O + ((size_t)qt * 64 + 2 * head) * 1024 + lane * 16;
#pragma unroll
          for (int s = 0; s < 2; ++s) ST16(op + s * 1024, pack8(o, s, sc)); }
        bars(2);
        drain();
        bar();
        if (w == 0) {
            if (lane == 0) st_tag(myflag + 4096, tag);
            const int hd = lane & 31;
            const char* fpn = ws + WS_F + 4096 + (((hd & 3) * 8 + (tt >> 1)) * 8 + (hd >> 2)) * 8;
            bars(4);
            const u64 v = ld_tag(fpn);
            bars(2);
            tags_acquire(fpn, v, tag);
        } else bars(6);
        bars(2);
        DIAG_MARK(3);
        {
            const int wm = w >> 1, wn = w & 1;
            const int q4 = (lane & 15) >> 2, p4 = lane & 3, cg = (lane >> 4) & 1, pi4 = ((p4 & 1) << 1) | (p4 >> 1);
            lds_cptr ap[2], bp[2];
#pragma unroll
            for (int h = 0; h < 2; ++h) {
                ap[h] = lp + M_G0 + h * 2 * M_GS + wm * 4096;
                bp[h] = (lds_cptr)lds + M_G0 + h * 2 * M_GS + M_H + (8 * hh + q4) * 128 + ((wn ^ (q4 >> 1)) << 6) + cg * 32 + pi4 * 8;
            }
            f32x16 acc = (f32x16){};
            bf16x8 a0[4], b0[4], a1[4], b1[4];
#pragma unroll
            for (int i = 0; i < 4; ++i) { a1[i] = (bf16x8){}; b1[i] = (bf16x8){}; }
#define F_ITER(st, AP, BP, AC, BC) do { \
                bar0(); \
                _Pragma("unroll") for (int i = 0; i < 4; ++i) { \
                    AC[i] = ld128(ap[(st) >> 1] + ((st) & 1) * M_GS + i * 1024); BC[i] = ldtr(bp[(st) >> 1] + ((st) & 1) * M_GS + i * 2048); SBAR(); \
                    acc = MFMA32(BP[i], AP[i], acc); PIN(acc); SBAR(); } \
                } while (0)
            LGKM0();
            for (int it = 0; it < 4; ++it) { F_ITER(0, a1, b1, a0, b0); F_ITER(1, a0, b0, a1, b1); F_ITER(2, a1, b1, a0, b0); F_ITER(3, a0, b0, a1, b1); }
#pragma unroll
            for (int i = 0; i < 4; ++i) acc = MFMA32(b1[i], a1[i], acc);
            DIAG_MARK(4);
#pragma unroll
            for (int s = 0; s < 2; ++s) {
                const u32x4 ya = *(const LAS u32x4*)(lp + w * 2048 + s * 1024), yb = *(const LAS u32x4*)(lp + 16384 + w * 2048 + s * 1024);
#pragma unroll
                for (int i = 0; i < 4; ++i) {
                    acc[8 * s + 2 * i] += __uint_as_float(ya[i] << 16) + __uint_as_float(yb[i] << 16);
                    acc[8 * s + 2 * i + 1] += __uint_as_float(ya[i] & 0xffff0000u) + __uint_as_float(yb[i] & 0xffff0000u);
                }
            }
            u16* op = out + (size_t)(tt * 128 + wm * 32 + r) * 1024 + (2 * et + part) * 64 + wn * 32 + hh * 8;
#pragma unroll
            for (int s = 0; s < 2; ++s) ST16(op + s * 16, pack8(acc, s, 1.f));
        }
        if (blockIdx.x == 0 && w == 0 && lane == 0) st_tag(ws + WS_E, tag);
        DIAG_END(0, ws);
    }
}

extern "C" void kernel_launch(void* const* d_in, const int* in_sizes, int n_in, void* d_out, int out_size, void* d_ws, size_t ws_size,
                              hipStream_t stream) {
    (void)hipFuncSetAttribute((const void*)k_all, hipFuncAttributeMaxDynamicSharedMemorySize, M_LDS);
    k_all<<<256, 1024, M_LDS, stream>>>((const u16*)d_in[0], (const u16*)d_in[1], (const u16*)d_in[2], (const u16*)d_in[3], (const u16*)d_in[4],
                                        (const u16*)d_in[6], (const u16*)d_in[7], (const u16*)d_in[8], (char*)d_ws, (u16*)d_out);
}
```

```cpp
#include <hip/hip_runtime.h>
#include <hip/hip_bf16.h>

#define WARM 0
#define RTSTAMP 0
typedef unsigned short u16;
typedef short bf16x8 __attribute__((ext_vector_type(8)));
typedef short s16x4 __attribute__((ext_vector_type(4)));
typedef float f32x16 __attribute__((ext_vector_type(16)));
typedef float f32x4 __attribute__((ext_vector_type(4)));
typedef float f32x2 __attribute__((ext_vector_type(2)));
typedef unsigned u32x4 __attribute__((ext_vector_type(4)));
#define LAS __attribute__((address_space(3)))
typedef LAS const char* lds_cptr;

constexpr size_t WS_Q = 0;
constexpr size_t WS_KV = 4u << 20;
constexpr size_t WS_AB = 12u << 20;
constexpr size_t WS_O = 20u << 20;
constexpr size_t WS_Y = 24u << 20;
constexpr size_t WS_G = 40u << 20;
constexpr size_t WS_F = WS_G + 65536;
constexpr size_t WS_E = WS_F + 16384;

#define DIAG_BEGIN()
#define DIAG_MARK(i)
#define DIAG_END(kid, wsp)

#define MFMA32(a, b, c) __builtin_amdgcn_mfma_f32_32x32x16_bf16((a), (b), (c), 0, 0, 0)
#define MFMA16(a, b, c) __builtin_amdgcn_mfma_f32_16x16x32_bf16((a), (b), (c), 0, 0, 0)
#define SBAR() __builtin_amdgcn_sched_barrier(0)
#define PIN(x) asm volatile("" : "+v"(x))
#define EX2(v) __builtin_amdgcn_exp2f(v)
#define PB(p) __builtin_bit_cast(bf16x8, p)
#define LGKM0() __builtin_amdgcn_s_waitcnt(0xC07F)

__device__ __forceinline__ unsigned cvtpk(float lo, float hi) {
    typedef __bf16 bf16x2_t __attribute__((ext_vector_type(2)));
    return __builtin_bit_cast(unsigned, __builtin_convertvector((f32x2){lo, hi}, bf16x2_t)); }
__device__ __forceinline__ float bfr(float f) { return __uint_as_float(cvtpk(f, 0.f) << 16); }

__device__ __forceinline__ void glds16(unsigned voff, const void* sbase, unsigned ldsdst) {
    asm volatile("s_mov_b32 m0, %2\n\ts_nop 0\n\tglobal_load_lds_dwordx4 %0, %1" :: "v"(voff), "s"(sbase), "s"(ldsdst) : "memory");
}
#define DSC " sc1"
__device__ __forceinline__ void glds16d(unsigned voff, const void* sbase, unsigned ldsdst) {
    asm volatile("s_mov_b32 m0, %2\n\ts_nop 0\n\tglobal_load_lds_dwordx4 %0, %1" DSC :: "v"(voff), "s"(sbase), "s"(ldsdst) : "memory");
}
__device__ __forceinline__ void glds2d(unsigned v0, unsigned v1, const void* sbase, unsigned ldsdst) {
    asm volatile("s_mov_b32 m0, %3\n\ts_nop 0\n\tglobal_load_lds_dwordx4 %0, %2" DSC "\n\tglobal_load_lds_dwordx4 %1, %2 offset:1024" DSC
                 :: "v"(v0), "v"(v1), "s"(sbase), "s"(ldsdst) : "memory");
}
__device__ __forceinline__ void glds4d(unsigned v0, unsigned v1, unsigned v2, unsigned v3, const void* sbase, unsigned ldsdst) {
    asm volatile("s_mov_b32 m0, %5\n\ts_nop 0\n\tglobal_load_lds_dwordx4 %0, %4" DSC "\n\tglobal_load_lds_dwordx4 %1, %4 offset:1024" DSC "\n\t"
                 "global_load_lds_dwordx4 %2, %4 offset:2048" DSC "\n\tglobal_load_lds_dwordx4 %3, %4 offset:3072" DSC
                 :: "v"(v0), "v"(v1), "v"(v2), "v"(v3), "s"(sbase), "s"(ldsdst) : "memory");
}
__device__ __forceinline__ void glds2(unsigned v0, unsigned v1, const void* sbase, unsigned ldsdst) {
    asm volatile("s_mov_b32 m0, %3\n\ts_nop 0\n\tglobal_load_lds_dwordx4 %0, %2\n\tglobal_load_lds_dwordx4 %1, %2 offset:1024"
                 :: "v"(v0), "v"(v1), "s"(sbase), "s"(ldsdst) : "memory");
}
__device__ __forceinline__ void glds4(unsigned v0, unsigned v1, unsigned v2, unsigned v3, const void* sbase, unsigned ldsdst) {
    asm volatile("s_mov_b32 m0, %5\n\ts_nop 0\n\tglobal_load_lds_dwordx4 %0, %4\n\tglobal_load_lds_dwordx4 %1, %4 offset:1024\n\t"
                 "global_load_lds_dwordx4 %2, %4 offset:2048\n\tglobal_load_lds_dwordx4 %3, %4 offset:3072"
                 :: "v"(v0), "v"(v1), "v"(v2), "v"(v3), "s"(sbase), "s"(ldsdst) : "memory");
}
template <int N> __device__ __forceinline__ void wait_bar() {
    SBAR();
    asm volatile("s_waitcnt vmcnt(%0)\n\ts_barrier" :: "n"(N) : "memory");
    SBAR();
}
__device__ __forceinline__ void bar() { SBAR(); asm volatile("s_barrier" ::: "memory"); SBAR(); }
__device__ __forceinline__ void bar0() { SBAR(); asm volatile("s_waitcnt lgkmcnt(0)\n\ts_barrier" ::: "memory"); SBAR(); }
__device__ __forceinline__ void bar_l1() {
    SBAR();
    asm volatile("s_waitcnt lgkmcnt(1)\n\ts_barrier" ::: "memory");
    SBAR();
}
__device__ __forceinline__ bf16x8 ld128(lds_cptr p) { return *(const LAS bf16x8*)p; }
__device__ __forceinline__ bf16x8 ldtr(lds_cptr p) {
    s16x4 lo = __builtin_amdgcn_ds_read_tr16_b64_v4i16((LAS s16x4*)p);
    s16x4 hi = __builtin_amdgcn_ds_read_tr16_b64_v4i16((LAS s16x4*)(p + 512));
    return (bf16x8){lo[0], lo[1], lo[2], lo[3], hi[0], hi[1], hi[2], hi[3]};
}
__device__ __forceinline__ u32x4 pack8(const f32x16& a, int s, float sc) {
    u32x4 w;
    w[0] = cvtpk(a[8 * s + 0] * sc, a[8 * s + 1] * sc); w[1] = cvtpk(a[8 * s + 2] * sc, a[8 * s + 3] * sc);
    w[2] = cvtpk(a[8 * s + 4] * sc, a[8 * s + 5] * sc); w[3] = cvtpk(a[8 * s + 6] * sc, a[8 * s + 7] * sc);
    return w;
}
#define STF " sc1"
#define ST16(p, v) do { const u32x4 v_ = (v); asm volatile("global_store_dwordx4 %0, %1, off" STF "\n\ts_nop 1" :: "v"(p), "v"(v_) : "memory"); } while (0)
__device__ __forceinline__ int swap23(int i) { return (i & ~12) | ((i & 4) << 1) | ((i & 8) >> 1); }

__device__ __forceinline__ int lf_order(int tau, int a, int c, int G) {
    if (tau == 0) return a;
    const int g = (tau - 1) >> 2, pos = (tau - 1) & 3;
    if (g != G - 1 && pos == c) return 4 * (g + 1) + a;
    const int b = (g == G - 1 || pos < c) ? pos : pos - 1;
    return 4 * g + (b < a ? b : b + 1);
}
#define NX3(v) ((v) == 2 ? 0 : (v) + 1)
#define NX5(v) ((v) == 4 ? 0 : (v) + 1)

__device__ __forceinline__ int rswz(int row) { return ((row >> 1) & 1) | (((row >> 3) & 3) << 1); }
__device__ __forceinline__ u32x4 pack44(const f32x4& a, const f32x4& b, float sc) {
    return (u32x4){cvtpk(a[0] * sc, a[1] * sc), cvtpk(a[2] * sc, a[3] * sc), cvtpk(b[0] * sc, b[1] * sc), cvtpk(b[2] * sc, b[3] * sc)};
}
__device__ __forceinline__ void drain() { asm volatile("s_waitcnt vmcnt(0)" ::: "memory"); }
typedef unsigned long long u64;
__device__ __forceinline__ u64 ld_tag(const char* p) { return __hip_atomic_load((const u64*)p, __ATOMIC_RELAXED, __HIP_MEMORY_SCOPE_AGENT); }
__device__ __forceinline__ void st_tag(char* p, u64 v) { __hip_atomic_store((u64*)p, v, __ATOMIC_RELAXED, __HIP_MEMORY_SCOPE_AGENT); }
__device__ __forceinline__ void tags_acquire(const char* p, u64 v, u64 tag) {
#pragma nounroll
    for (int spin = 0; !__all(v == tag) && spin < (1 << 22); ++spin) { __builtin_amdgcn_s_sleep(2); v = ld_tag(p); }
    asm volatile("" ::: "memory");
}
__device__ __forceinline__ u64 call_tag(const char* ws) {
    const u64 e = ld_tag(ws + WS_E) + 1;
    return ((u64)(unsigned)__builtin_amdgcn_readfirstlane((int)(e >> 32)) << 32) | (unsigned)__builtin_amdgcn_readfirstlane((int)e);
}
__device__ __forceinline__ int fresh_lane() {
    int l; asm volatile("v_mbcnt_lo_u32_b32 %0, -1, 0\n\tv_mbcnt_hi_u32_b32 %0, -1, %0" : "=v"(l)); return l;
}
__device__ __forceinline__ float DOT2(unsigned a, unsigned b, float acc) {
    typedef __bf16 bf2_t __attribute__((ext_vector_type(2)));
    return __builtin_amdgcn_fdot2_f32_bf16(__builtin_bit_cast(bf2_t, a), __builtin_bit_cast(bf2_t, b), acc, false);
}
__device__ __forceinline__ void bars(int n) { for (int i = 0; i < n; ++i) bar(); }

constexpr int M_KS = 8192, M_G0 = 4 * M_KS, M_GS = 32768, M_H = 16384, M_LDS = M_G0 + 4 * M_GS;
constexpr int CODE_KB = 23;
constexpr int NA = 32, NY = 44, NO = 76;
__global__ __launch_bounds__(1024, 4) void k_all(const u16* __restrict__ x, const u16* __restrict__ cr, const u16* __restrict__ ci,
                                                  const u16* __restrict__ hr, const u16* __restrict__ hi, const u16* __restrict__ wqkv,
                                                  const u16* __restrict__ wo, const u16* __restrict__ gw, char* ws, u16* __restrict__ out) {
    extern __shared__ __attribute__((aligned(16))) char smem[];
    LAS char* lds = (LAS char*)smem;
    unsigned long long pc0; asm volatile("s_getpc_b64 %0" : "=s"(pc0));
    DIAG_BEGIN();
    const int w = (__builtin_amdgcn_readfirstlane(threadIdx.x >> 6) + 12) & 15;
    const int xcd = blockIdx.x & 7, idx = blockIdx.x >> 3;
    const int rt = (xcd & 1) * 8 + (idx & 7), j = (xcd >> 1) * 4 + (idx >> 3);
    const int head = xcd * 4 + (idx >> 3), qb = idx & 7;
    const int part = xcd >> 2, tt = ((xcd >> 1) & 1) * 8 + (idx & 7), et = (xcd & 1) * 4 + (idx >> 3);
    const char* kb = ws + WS_KV + (size_t)head * 131072;
#define LANE_VARS() const int lane = fresh_lane(), r = lane & 31, hh = lane >> 5, c = lane & 15, g4 = lane >> 4, sg0 = 8 * (c >> 2) + (c & 3); \
    lds_cptr lp = (lds_cptr)lds + lane * 16; (void)r; (void)hh; (void)g4; (void)sg0; (void)lp
    char* myflag = ws + WS_F + blockIdx.x * 8;
    if (w >= 12) {
        LANE_VARS();
        const int g = w - 12;
        const int la = ((idx >> 3) - (idx & 7)) & 3, lc = (idx >> 1) & 3;
        const int tab16 = lf_order(lane & 15, la, lc, 4), tab32 = lf_order(lane & 31, la, lc, 8);
        const unsigned lds0 = (unsigned)(uintptr_t)lds;
        const unsigned vL = (unsigned)(lane * 16);
        unsigned vR[4], vW[2], vC[4], vA[2], vT[2];
#pragma unroll
        for (int i = 0; i < 4; ++i) {
            vR[i] = (unsigned)((8 * i + (lane >> 3)) * 2048 + (((lane & 7) ^ rswz(8 * i + (lane >> 3))) << 4) - i * 1024);
            vC[i] = (unsigned)((8 * i + (lane >> 3)) * 4096 + (((lane & 7) ^ ((4 * i + (lane >> 4)) & 7)) << 4) - i * 1024);
        }
#pragma unroll
        for (int i = 0; i < 2; ++i) {
            vW[i] = (unsigned)((8 * i + (lane >> 3)) * 6144 + (((lane & 7) ^ (((lane >> 4) & 1) << 2) ^ (i << 1)) << 4) - i * 1024);
            vT[i] = (unsigned)((8 * i + (lane >> 3)) * 2048 + (((lane & 7) ^ (((lane >> 4) & 1) << 2)) << 4) - i * 1024);
            vA[i] = (unsigned)(lane * 16 + i * (65536 - 2048));
        }
        const unsigned vKV = (unsigned)(lane * 16 + (4 << 20) - 3072 - 1024);
        const char* xb = (const char*)x + (size_t)(rt * 128 + g * 32) * 2048;
        const char* hb = (const char*)(j < 8 ? hr : hi) + (size_t)((j & 7) * 128 + g * 32) * 2048;
        const char* wb = (const char*)wqkv + (size_t)g * 16 * 6144 + j * 128;
        const char* cb = (const char*)(part ? ci : cr) + (size_t)(tt * 128 + 32 * g) * 4096;
        const char* ab = ws + WS_AB + (size_t)(part * 64 + et * 8 + 2 * g) * 65536;
        const char* ob = ws + WS_O + (size_t)(tt * 4 + g) * 65536;
        const char* wob = (const char*)wo + (2 * et + part) * 128;
        const unsigned dG = lds0 + M_G0 + g * 4096, dK = lds0 + g * 2048;
        auto sP = [&](int n) {
            const int kp = __builtin_amdgcn_readlane(tab16, n); const unsigned so = (n & 3) * M_GS;
            const char* b = wb + (size_t)(kp * (64 * 6144));
            glds4(vR[0], vR[1], vR[2], vR[3], xb + kp * 128, dG + so);
            glds2(vW[0], vW[1], b, dG + so + M_H - g * 2048); glds2(vW[0], vW[1], b + 2048, dG + so + M_H + 8192 - g * 2048);
            glds2(vW[0], vW[1], b + 4096, dK + (n & 3) * M_KS);
        };
        auto sH = [&](int n) {
            const int kp = __builtin_amdgcn_readlane(tab16, n & 15); const unsigned so = (n & 3) * M_GS;
            glds4(vR[0], vR[1], vR[2], vR[3], xb + kp * 128, dG + so); glds4(vR[0], vR[1], vR[2], vR[3], hb + kp * 128, dG + so + M_H);
        };
        auto sKV = [&](int n) {
            glds2d(vL, vKV, kb + ((n - NA) * 4096 + 4096 + g * 1024), dK + (n & 3) * M_KS);
        };
        auto sY = [&](int n) {
            const int kp = __builtin_amdgcn_readlane(tab32, n - NY); const unsigned so = (n & 3) * M_GS;
            glds4(vC[0], vC[1], vC[2], vC[3], cb + kp * 128, dG + so); glds4d(vA[0], vA[0], vA[1], vA[1], ab + kp * 2048, dG + so + M_H);
        };
        auto sO = [&](int n) {
            const int kp = __builtin_amdgcn_readlane(tab16, n - NO); const unsigned so = (n & 3) * M_GS;
            glds2(vT[0], vT[1], wob + (size_t)((kp * 64 + g * 16) * 2048), dG + so + M_H - g * 2048);
            glds4d(vL, vL, vL, vL, ob + kp * 4096, dG + so);
        };
#define STAMP()
#define STAMP_DUMP()
#define WB(N) do { wait_bar<N>(); STAMP(); } while (0)
        auto sC = [&](int n) { const int kp = __builtin_amdgcn_readlane(tab32, n - NY); glds4(vC[0], vC[1], vC[2], vC[3], cb + kp * 128, dG + (n & 3) * M_GS); };
        auto sAB = [&](int n) { const int kp = __builtin_amdgcn_readlane(tab32, n - NY); glds4d(vA[0], vA[0], vA[1], vA[1], ab + kp * 2048, dG + (n & 3) * M_GS + M_H); };
        sP(0); sP(1); WB(10);
        sP(2);
        for (int k = 1; k < 14; ++k) { sP(k + 2); WB(20); }
        sH(16); WB(18);
        for (int k = 15; k < 30; ++k) { sH(k + 2); WB(16); }
        glds16d(vL, kb + g * 1024, dK + 3 * M_KS); glds16d(vL, kb + (4 << 20), dK + 3 * M_KS + 1024);
        WB(10);
        for (int k = 31; k < 36; ++k) { sKV(k + 1); WB(2); }
        sKV(37); sC(44); WB(6);
        for (int k = 37; k < 40; ++k) { sKV(k + 1); sC(k + 8); WB(10); }
        sKV(41); WB(6);
        sKV(42); WB(2);
        sKV(43); WB(2);
        sKV(44); sAB(44); sAB(45); WB(10);
        sKV(45); sAB(46); WB(10);
        sKV(46); sAB(47); WB(10);
        sKV(47); sY(48); WB(14);
        for (int k = 47; k < 62; ++k) { sKV(k + 1); sY(k + 2); WB(18); }
        { const int vo = g < 3 ? 31 * 4096 + 1024 + g * 1024 : 0;
          glds16d(vL, kb + (4 << 20) + vo, dK + 3 * M_KS + 1024); sY(64); WB(17); }
        for (int k = 63; k < 70; ++k) { sY(k + 2); WB(16); }
        glds16d(vL, ws + WS_G + (size_t)tt * 1024, lds0 + 16384);
        sY(72); WB(17);
        sY(73); WB(17);
        sY(74); WB(16);
        sY(75); WB(16);
        WB(8);
        sO(76);
        for (int k = 75; k < 89; ++k) { sO(k + 2); WB(12); }
        sO(91); glds4d(vL, vL, vL, vL, ws + WS_Y + (size_t)(blockIdx.x ^ 4) * 16384 + g * 4096, lds0 + 16384 + g * 4096);
        WB(16);
        WB(10);
        WB(0);
        static_assert(NA == 32 && NY == 44 && NO == 76, "the DMA program is written for this schedule");
        STAMP_DUMP();
    } else if (w >= 8) {
        LANE_VARS();
        const int g = w - 8, gm = g >> 1, gn = g & 1;
        { const int piece = (idx & 7) * 4 + g;
          if (piece < CODE_KB) { u32x4 v; asm volatile("global_load_dwordx4 %0, %1, %2\n\ts_waitcnt vmcnt(0)" : "=&v"(v) : "v"(piece * 1024 + lane * 16), "s"(pc0 & ~255ull) : "memory"); } }
        const u64 tag = call_tag(ws);
        __builtin_amdgcn_s_setprio(3);
        bars(2);
        {
            const u32x4* xr = (const u32x4*)(x + (size_t)(blockIdx.x * 8 + 2 * g) * 1024); const u32x4* gr = (const u32x4*)gw;
            const u32x4 a0 = xr[lane], a1 = xr[lane + 64], a2 = xr[lane + 128], a3 = xr[lane + 192], b0 = gr[lane], b1 = gr[lane + 64];
            bars(4);
            float z0 = 0.f, z1 = 0.f;
#pragma unroll
            for (int e = 0; e < 4; ++e) { z0 = DOT2(a0[e], b0[e], z0); z1 = DOT2(a2[e], b0[e], z1); z0 = DOT2(a1[e], b1[e], z0); z1 = DOT2(a3[e], b1[e], z1); }
#define DPP_ADD(v, ctrl, rmask) v += __uint_as_float(__builtin_amdgcn_update_dpp(0u, __float_as_uint(v), ctrl, rmask, 0xf, false))
            DPP_ADD(z0, 0x111, 0xf); DPP_ADD(z1, 0x111, 0xf); DPP_ADD(z0, 0x112, 0xf); DPP_ADD(z1, 0x112, 0xf);
            DPP_ADD(z0, 0x114, 0xf); DPP_ADD(z1, 0x114, 0xf); DPP_ADD(z0, 0x118, 0xf); DPP_ADD(z1, 0x118, 0xf);
            DPP_ADD(z0, 0x142, 0xa); DPP_ADD(z1, 0x142, 0xa); DPP_ADD(z0, 0x143, 0xc); DPP_ADD(z1, 0x143, 0xc);
            z0 = __uint_as_float(__builtin_amdgcn_readlane(__float_as_uint(z0), 63)); z1 = __uint_as_float(__builtin_amdgcn_readlane(__float_as_uint(z1), 63));
            bar();
            const float gt = bfr(1.f / (1.f + __expf(-bfr((lane & 1) ? z1 : z0))));
            if (lane < 2) st_tag(ws + WS_G + (size_t)(blockIdx.x * 8 + 2 * g + lane) * 8, ((u64)__float_as_uint(bfr(1.f - gt)) << 32) | __float_as_uint(gt));
            bars(6);
            __builtin_amdgcn_s_setprio(0);
            bars(2);
        }
        f32x4 y[4][4];
        bf16x8 cA[4], cB[4], fa[4];
        lds_cptr ca[2][2], fp[2][2];
#define G_MM(SW, a_, CX, m_) y[a_][m_] = (SW) ? MFMA16(CX[m_], fa[a_], y[a_][m_]) : MFMA16(fa[a_], CX[m_], y[a_][m_])
#define G_STEP(SW, CP, CC, ST, HF, CO, FO) do { \
            _Pragma("unroll") for (int s_ = 0; s_ < 4; ++s_) { \
                _Pragma("unroll") for (int m = 0; m < 4; ++m) { \
                    if (s_ < 2) G_MM(SW, s_ + 2, CP, m); else G_MM(SW, s_ - 2, CC, m); \
                    if (s_ == 0) CC[m] = ld128(ca[(ST) >> 1][HF] + ((ST) & 1) * M_GS + CO(m)); \
                    if (m == 0 && s_ < 3) fa[s_] = ld128(fp[(ST) >> 1][HF] + ((ST) & 1) * M_GS + FO(s_)); \
                    if (m == 2 && s_ == 2) fa[3] = ld128(fp[(ST) >> 1][HF] + ((ST) & 1) * M_GS + FO(3)); \
                    SBAR(); } } } while (0)
#define G_ITER(SW, st, CO, FO) do { bar0(); G_STEP(SW, cB, cA, st, 0, CO, FO); G_STEP(SW, cA, cB, st, 1, CO, FO); } while (0)
#define G_RUN(SW, NIT, CO, FO, PRE) do { \
            _Pragma("unroll") for (int a = 0; a < 4; ++a) { cB[a] = (bf16x8){}; fa[a] = (bf16x8){}; \
                _Pragma("unroll") for (int b = 0; b < 4; ++b) y[a][b] = (f32x4){}; } \
            PRE; LGKM0(); \
            for (int it = 0; it < NIT; ++it) { G_ITER(SW, 0, CO, FO); G_ITER(SW, 1, CO, FO); G_ITER(SW, 2, CO, FO); G_ITER(SW, 3, CO, FO); } \
            bar0(); \
            _Pragma("unroll") for (int a = 2; a < 4; ++a) _Pragma("unroll") for (int m = 0; m < 4; ++m) G_MM(SW, a, cB, m); } while (0)
#define OFF_SIG(i) (((i) >> 1) * 4096 + ((i) & 1) * 512)
#define OFF_2K(i) ((i) * 2048)
#pragma unroll
        for (int h = 0; h < 2; ++h)
#pragma unroll
            for (int hf = 0; hf < 2; ++hf) {
                const int ra = M_G0 + h * 2 * M_GS + sg0 * 128 + (((4 * hf + g4) ^ rswz(sg0)) << 4);
                ca[h][hf] = (lds_cptr)lds + ra + gm * 8192; fp[h][hf] = (lds_cptr)lds + ra + M_H + gn * 8192; PIN(ca[h][hf]); PIN(fp[h][hf]);
            }
        G_RUN(true, 4, OFF_SIG, OFF_SIG, drain(); bar());
      { LANE_VARS();
#pragma unroll
        for (int gr = 0; gr < 2; ++gr) {
            char* abp = ws + WS_AB + ((size_t)(j * 8 + gn * 4) * 64 + rt * 4 + gm * 2 + gr) * 1024 + lane * 16;
#pragma unroll
            for (int b = 0; b < 4; ++b) ST16(abp + (size_t)b * 65536, pack44(y[b][2 * gr], y[b][2 * gr + 1], 1.f));
            bar();
        }
        bar();
        drain();
        bar();
        if (g == 0) {
            if (lane == 0) st_tag(myflag + 2048, tag);
            const int jj = part * 8 + et, rr = lane & 15;
            const char* fpn = ws + WS_F + 2048 + (((rr & 7) | ((jj & 3) << 3)) * 8 + ((rr >> 3) | ((jj >> 2) << 1))) * 8;
            bars(4);
            const u64 v = ld_tag(fpn);
            bar();
            tags_acquire(fpn, v, tag);
        } else bars(5);
        bar();
#pragma unroll
        for (int h = 0; h < 2; ++h)
#pragma unroll
            for (int hf = 0; hf < 2; ++hf) {
                ca[h][hf] = (lds_cptr)lds + M_G0 + h * 2 * M_GS + (gm * 64 + c) * 128 + (((4 * hf + g4) ^ ((c >> 1) & 7)) << 4);
                fp[h][hf] = lp + M_G0 + h * 2 * M_GS + M_H + gn * 8192 + hf * 1024; PIN(ca[h][hf]); PIN(fp[h][hf]);
            }
      }
        G_RUN(false, 8, OFF_2K, OFF_2K, bar());
      { LANE_VARS();
        __builtin_amdgcn_s_setprio(3);
#pragma unroll
        for (int m = 0; m < 4; ++m) {
            float gy = *(const LAS float*)(lds + 16384 + (gm * 64 + m * 16 + c) * 8);
            if (part) gy = -gy;
#pragma unroll
            for (int e = 0; e < 2; ++e) {
                const int off = ((gm * 2 + (m >> 1)) * 2 + e) * 2048 + (g4 >> 1) * 1024 + ((g4 & 1) * 32 + (m & 1) * 16 + c) * 16;
                const u32x4 v = pack44(y[2 * e][m], y[2 * e + 1][m], gy);
                if (gn == part) *(LAS u32x4*)(lds + off) = v; else ST16(ws + WS_Y + (size_t)blockIdx.x * 16384 + off, v);
            }
            bar();
        }
        bars(2);
        asm volatile("s_waitcnt vmcnt(0) lgkmcnt(0)" ::: "memory");
        bar();
        if (g == 0) {
            if (lane == 0) st_tag(myflag + 6144, tag);
            const char* fpn = ws + WS_F + 6144 + (blockIdx.x ^ 4) * 8;
            bars(3);
            const u64 v = ld_tag(fpn);
            bar();
            tags_acquire(fpn, v, tag);
            bar();
        }
      }
    } else {
        LANE_VARS();
        const u64 tag = call_tag(ws);
        {
            const int wm = w >> 2, wn = (w >> 1) & 1, wq = w & 1;
            lds_cptr xa[2][2], tq[2][2], tv;
            { const int q4 = c >> 2, p4 = c & 3;
#pragma unroll
              for (int i = 0; i < 2; ++i) {
                  const int tbo = (8 * g4 + q4) * 128 + ((((wn * 4 + i * 2) ^ ((q4 >> 1) << 2) ^ ((g4 & 1) << 1)) + (p4 >> 1)) << 4) + (p4 & 1) * 8;
#pragma unroll
                  for (int h = 0; h < 2; ++h) {
                      xa[h][i] = (lds_cptr)lds + M_G0 + h * 2 * M_GS + wm * 8192 + sg0 * 128 + (((4 * i + g4) ^ rswz(sg0)) << 4); PIN(xa[h][i]);
                      tq[h][i] = (lds_cptr)lds + M_G0 + h * 2 * M_GS + M_H + wq * 8192 + tbo; PIN(tq[h][i]);
                  } }
              tv = (lds_cptr)lds + (8 * g4 + q4) * 128 + ((((wn * 4 + wq * 2) ^ ((q4 >> 1) << 2) ^ ((g4 & 1) << 1)) + (p4 >> 1)) << 4) + (p4 & 1) * 8; PIN(tv); }
            f32x4 acc[3][4];
            bf16x8 xA[4], xB[4], fb[3];
#pragma unroll
            for (int m = 0; m < 4; ++m) { acc[0][m] = (f32x4){}; acc[1][m] = (f32x4){}; acc[2][m] = (f32x4){}; xB[m] = (bf16x8){}; }
            fb[1] = fb[2] = (bf16x8){};
#define Q_MM(b_, XF, m_) acc[b_][m_] = (b_) < 2 ? MFMA16(fb[b_], XF[m_], acc[b_][m_]) : MFMA16(XF[m_], fb[b_], acc[b_][m_])
#define Q_STEP(XP, XC, ST, HF) do { \
                _Pragma("unroll") for (int s_ = 0; s_ < 3; ++s_) { \
                    _Pragma("unroll") for (int m = 0; m < 4; ++m) { \
                        if (s_ < 2) Q_MM(s_ + 1, XP, m); else Q_MM(0, XC, m); \
                        SBAR(); \
                        if (s_ < 2 && m >= 2) XC[2 * s_ + m - 2] = ld128(xa[(ST) >> 1][HF] + ((ST) & 1) * M_GS + s_ * 4096 + (m - 2) * 512); \
                        if (m == 0) fb[s_] = ldtr((HF) * 4096 + (s_ < 2 ? tq[(ST) >> 1][s_ & 1] + ((ST) & 1) * M_GS : tv + (ST) * M_KS)); \
                        SBAR(); } } } while (0)
#define Q_ITER(st) do { bar0(); Q_STEP(xB, xA, st, 0); Q_STEP(xA, xB, st, 1); } while (0)
            LGKM0();
            for (int it = 0; it < 4; ++it) { Q_ITER(0); Q_ITER(1); Q_ITER(2); Q_ITER(3); }
            bar0();
#pragma unroll
            for (int b = 1; b < 3; ++b)
#pragma unroll
                for (int m = 0; m < 4; ++m) Q_MM(b, xB, m);
            DIAG_MARK(0);
            const int hd = 2 * j + wn;
            const float qs = wq ? 1.f : 0.17677669529663687f * 1.4426950408889634f;
            char* bp = ws + (size_t)(hd * 64 + rt * 4 + wm * 2) * 2048 + (g4 >> 1) * 1024 + (g4 & 1) * 512;
            char* qk = bp + (wq ? WS_KV + (16 * (c >> 3) + (c & 7)) * 16 : WS_Q + sg0 * 16);
            const int as = wq ? 128 : 64;
#pragma unroll
            for (int gr = 0; gr < 2; ++gr) { ST16(bp + WS_KV + (4u << 20) + gr * 2048 + (16 * wq + swap23(c)) * 16, pack44(acc[2][2 * gr], acc[2][2 * gr + 1], 1.f)); }
            bar();
#pragma unroll
            for (int a = 0; a < 2; ++a) ST16(qk + a * as, pack44(acc[0][a], acc[1][a], qs));
            bar();
#pragma unroll
            for (int a = 0; a < 2; ++a) ST16(qk + 2048 + a * as, pack44(acc[0][2 + a], acc[1][2 + a], qs));
        }
        bars(3);
        drain();
        bar();
        if (w == 0) {
            if (lane == 0) st_tag(myflag, tag);
            const int jj = head >> 1, rr = lane & 15;
            const int pb = lane < 16 ? ((rr & 7) | ((jj & 3) << 3)) * 8 + ((rr >> 3) | ((jj >> 2) << 1)) : (lane < 32 ? tt * 16 + rr : qb * 32 + (lane - 32));
            const char* fpn = ws + WS_F + pb * 8;
            bars(4);
            const u64 v = ld_tag(fpn);
            bars(2);
            tags_acquire(fpn, v, tag);
        } else bars(6);
        bar();
        const int qt = qb * 8 + w;
        bf16x8 qf[2]; float omg;
        { const char* qp = ws + WS_Q + (size_t)(head * 64 + qt) * 2048 + lane * 16;
          asm volatile("global_load_dwordx4 %0, %1, off sc1" : "=v"(qf[0]) : "v"(qp) : "memory");
          asm volatile("global_load_dwordx4 %0, %1, off offset:1024 sc1" : "=v"(qf[1]) : "v"(qp) : "memory");
          asm volatile("global_load_dword %0, %1, off offset:4 sc1" : "=v"(omg) : "v"(ws + WS_G + (size_t)(qt * 32 + r) * 8) : "memory"); }
        bf16x8 sel; { const short one = (((lane & 1) == ((lane >> 4) & 1))) ? (short)0x3f80 : (short)0;
#pragma unroll
            for (int i = 0; i < 8; ++i) sel[i] = one; }
        f32x16 o = (f32x16){}; f32x4 l4 = (f32x4){};
        f32x16 negm, sA, sB;
        bf16x8 kf[2], vf0, vf1;
        u32x4 p0, p1;
        bars(2);
        asm volatile("s_waitcnt vmcnt(0)" : "+v"(qf[0]), "+v"(qf[1]), "+v"(omg) :: "memory");
        DIAG_MARK(1);
        kf[0] = ld128(lp + 3 * M_KS); kf[1] = ld128(lp + 3 * M_KS + 2048);
        sA = MFMA32(kf[0], qf[0], (f32x16){}); sA = MFMA32(kf[1], qf[1], sA);
        { float m = sA[0];
#pragma unroll
          for (int i = 1; i < 16; ++i) m = fmaxf(m, sA[i]);
          m = fmaxf(m, __shfl_xor(m, 32));
#pragma unroll
          for (int i = 0; i < 16; ++i) { negm[i] = -m; sA[i] -= m; } }
        kf[0] = ld128(lp + 3 * M_KS + 4096); kf[1] = ld128(lp + 3 * M_KS + 6144);
        vf0 = ld128(lp + 3 * M_KS + 1024);
        vf1 = vf0; p1 = (u32x4){0u, 0u, 0u, 0u};
#define EN(S, i, n) do { _Pragma("unroll") for (int e_ = 0; e_ < n; ++e_) S[i + e_] = EX2(S[i + e_]); } while (0)
#define A_STEP(SC, SN, v1o, kn, v0n) do { \
            SN = MFMA32(kf[0], qf[0], negm); PIN(SN); SBAR(); \
            EN(SC, 0, 3); PIN(SC); SBAR(); \
            o = MFMA32(vf1, PB(p1), o); PIN(o); SBAR(); \
            EN(SC, 3, 3); PIN(SC); SBAR(); \
            SN = MFMA32(kf[1], qf[1], SN); PIN(SN); SBAR(); \
            vf1 = ld128(lp + (v1o)); kf[0] = ld128(lp + (kn)); kf[1] = ld128(lp + (kn) + 2048); SBAR(); \
            EN(SC, 6, 2); p0[0] = cvtpk(SC[0], SC[1]); p0[1] = cvtpk(SC[2], SC[3]); PIN(SC); PIN(p0); SBAR(); \
            l4 = MFMA16(sel, PB(p1), l4); PIN(l4); SBAR(); \
            p0[2] = cvtpk(SC[4], SC[5]); p0[3] = cvtpk(SC[6], SC[7]); PIN(p0); SBAR(); EN(SC, 8, 2); PIN(SC); SBAR(); \
            o = MFMA32(vf0, PB(p0), o); PIN(o); SBAR(); \
            vf0 = ld128(lp + (v0n)); SBAR(); \
            EN(SC, 10, 3); PIN(SC); SBAR(); \
            l4 = MFMA16(sel, PB(p0), l4); PIN(l4); SBAR(); \
            EN(SC, 13, 3); \
            _Pragma("unroll") for (int i = 0; i < 4; ++i) p1[i] = cvtpk(SC[8 + 2 * i], SC[9 + 2 * i]); \
            PIN(p1); SBAR(); \
        } while (0)
#define A_ITER(st) do { \
            constexpr int b0 = (st) * M_KS; \
            bar(); \
            A_STEP(sA, sB, b0 + 1024, b0, b0 + 3072); \
            A_STEP(sB, sA, b0 + 5120, b0 + 4096, b0 + 7168); \
            } while (0)
        static_assert(NA % 4 == 0 && NY % 4 == 0 && NO % 4 == 0, "slot phase");
        LGKM0();
        for (int it = 0; it < 8; ++it) { A_ITER(0); A_ITER(1); A_ITER(2); A_ITER(3); }
        bar();
        o = MFMA32(vf1, PB(p1), o); l4 = MFMA16(sel, PB(p1), l4);
        DIAG_MARK(2);
        float l = ((lane >> 4) & 1) ? l4[1] : l4[0];
        bool ok = l < 1.0e30f;
#pragma unroll
        for (int i = 0; i < 16; ++i) ok = ok && (fabsf(o[i]) < 3.0e38f);
        if (!__all(ok)) {
            float m = -INFINITY, ls = 0.f; o = (f32x16){};
            const char* gp = kb + lane * 16;
            for (int t = 0; t < 64; ++t) {
                bf16x8 k0, k1, v0, v1;
                asm volatile("global_load_dwordx4 %0, %4, off sc1\n\tglobal_load_dwordx4 %1, %4, off offset:1024 sc1\n\t"
                             "global_load_dwordx4 %2, %5, off sc1\n\tglobal_load_dwordx4 %3, %5, off offset:1024 sc1\n\ts_waitcnt vmcnt(0)"
                             : "=&v"(k0), "=&v"(k1), "=&v"(v0), "=&v"(v1) : "v"(gp + t * 2048), "v"(gp + (4 << 20) + t * 2048) : "memory");
                f32x16 s = MFMA32(k0, qf[0], (f32x16){}); s = MFMA32(k1, qf[1], s);
                float tm = s[0];
#pragma unroll
                for (int i = 1; i < 16; ++i) tm = fmaxf(tm, s[i]);
                tm = fmaxf(tm, __shfl_xor(tm, 32));
                const float mn = fmaxf(m, tm), al = EX2(m - mn);
                m = mn; ls *= al;
#pragma unroll
                for (int i = 0; i < 16; ++i) { o[i] *= al; s[i] = EX2(s[i] - mn); ls += s[i]; }
                u32x4 a0, a1;
#pragma unroll
                for (int i = 0; i < 4; ++i) { a0[i] = cvtpk(s[2 * i], s[2 * i + 1]); a1[i] = cvtpk(s[8 + 2 * i], s[9 + 2 * i]); }
                o = MFMA32(v0, PB(a0), o); o = MFMA32(v1, PB(a1), o);
            }
            l = ls + __shfl_xor(ls, 32);
        }
        { const float sc = omg / l;
          char* op = ws + WS_O + ((size_t)qt * 64 + 2 * head) * 1024 + lane * 16;
#pragma unroll
          for (int s = 0; s < 2; ++s) ST16(op + s * 1024, pack8(o, s, sc)); }
        bars(2);
        drain();
        bar();
        if (w == 0) {
            if (lane == 0) st_tag(myflag + 4096, tag);
            const int hd = lane & 31;
            const char* fpn = ws + WS_F + 4096 + (((hd & 3) * 8 + (tt >> 1)) * 8 + (hd >> 2)) * 8;
            bars(4);
            const u64 v = ld_tag(fpn);
            bars(2);
            tags_acquire(fpn, v, tag);
        } else bars(6);
        bars(2);
        DIAG_MARK(3);
        {
            const int wm = w >> 1, wn = w & 1;
            const int q4 = (lane & 15) >> 2, p4 = lane & 3, cg = (lane >> 4) & 1, pi4 = ((p4 & 1) << 1) | (p4 >> 1);
            lds_cptr ap[2], bp[2];
#pragma unroll
            for (int h = 0; h < 2; ++h) {
                ap[h] = lp + M_G0 + h * 2 * M_GS + wm * 4096;
                bp[h] = (lds_cptr)lds + M_G0 + h * 2 * M_GS + M_H + (8 * hh + q4) * 128 + ((wn ^ (q4 >> 1)) << 6) + cg * 32 + pi4 * 8;
            }
            f32x16 acc = (f32x16){};
            bf16x8 a0[4], b0[4], a1[4], b1[4];
#pragma unroll
            for (int i = 0; i < 4; ++i) { a1[i] = (bf16x8){}; b1[i] = (bf16x8){}; }
#define F_ITER(st, AP, BP, AC, BC) do { \
                bar0(); \
                _Pragma("unroll") for (int i = 0; i < 4; ++i) { \
                    AC[i] = ld128(ap[(st) >> 1] + ((st) & 1) * M_GS + i * 1024); BC[i] = ldtr(bp[(st) >> 1] + ((st) & 1) * M_GS + i * 2048); SBAR(); \
                    acc = MFMA32(BP[i], AP[i], acc); PIN(acc); SBAR(); } \
                } while (0)
            LGKM0();
            for (int it = 0; it < 4; ++it) { F_ITER(0, a1, b1, a0, b0); F_ITER(1, a0, b0, a1, b1); F_ITER(2, a1, b1, a0, b0); F_ITER(3, a0, b0, a1, b1); }
#pragma unroll
            for (int i = 0; i < 4; ++i) acc = MFMA32(b1[i], a1[i], acc);
            DIAG_MARK(4);
#pragma unroll
            for (int s = 0; s < 2; ++s) {
                const u32x4 ya = *(const LAS u32x4*)(lp + w * 2048 + s * 1024), yb = *(const LAS u32x4*)(lp + 16384 + w * 2048 + s * 1024);
#pragma unroll
                for (int i = 0; i < 4; ++i) {
                    acc[8 * s + 2 * i] += __uint_as_float(ya[i] << 16) + __uint_as_float(yb[i] << 16);
                    acc[8 * s + 2 * i + 1] += __uint_as_float(ya[i] & 0xffff0000u) + __uint_as_float(yb[i] & 0xffff0000u);
                }
            }
            u16* op = out + (size_t)(tt * 128 + wm * 32 + r) * 1024 + (2 * et + part) * 64 + wn * 32 + hh * 8;
#pragma unroll
            for (int s = 0; s < 2; ++s) ST16(op + s * 16, pack8(acc, s, 1.f));
        }
        if (blockIdx.x == 0 && w == 0 && lane == 0) st_tag(ws + WS_E, tag);
        DIAG_END(0, ws);
    }
}

extern "C" void kernel_launch(void* const* d_in, const int* in_sizes, int n_in, void* d_out, int out_size, void* d_ws, size_t ws_size,
                              hipStream_t stream) {
    (void)hipFuncSetAttribute((const void*)k_all, hipFuncAttributeMaxDynamicSharedMemorySize, M_LDS);
    k_all<<<256, 1024, M_LDS, stream>>>((const u16*)d_in[0], (const u16*)d_in[1], (const u16*)d_in[2], (const u16*)d_in[3], (const u16*)d_in[4],
                                        (const u16*)d_in[6], (const u16*)d_in[7], (const u16*)d_in[8], (char*)d_ws, (u16*)d_out);
}
```

```cpp
#include <hip/hip_runtime.h>
#include <hip/hip_bf16.h>

#define QD 1
typedef unsigned short u16;
typedef short bf16x8 __attribute__((ext_vector_type(8)));
typedef short s16x4 __attribute__((ext_vector_type(4)));
typedef float f32x16 __attribute__((ext_vector_type(16)));
typedef float f32x4 __attribute__((ext_vector_type(4)));
typedef float f32x2 __attribute__((ext_vector_type(2)));
typedef unsigned u32x4 __attribute__((ext_vector_type(4)));
#define LAS __attribute__((address_space(3)))
typedef LAS const char* lds_cptr;

constexpr size_t WS_Q = 0;
constexpr size_t WS_KV = 4u << 20;
constexpr size_t WS_AB = 12u << 20;
constexpr size_t WS_O = 20u << 20;
constexpr size_t WS_Y = 24u << 20;
constexpr size_t WS_G = 40u << 20;
constexpr size_t WS_F = WS_G + 65536;
constexpr size_t WS_E = WS_F + 16384;

#define MFMA32(a, b, c) __builtin_amdgcn_mfma_f32_32x32x16_bf16((a), (b), (c), 0, 0, 0)
#define MFMA16(a, b, c) __builtin_amdgcn_mfma_f32_16x16x32_bf16((a), (b), (c), 0, 0, 0)
#define SBAR() __builtin_amdgcn_sched_barrier(0)
#define PIN(x) asm volatile("" : "+v"(x))
#define EX2(v) __builtin_amdgcn_exp2f(v)
#define PB(p) __builtin_bit_cast(bf16x8, p)
#define LGKM0() __builtin_amdgcn_s_waitcnt(0xC07F)

__device__ __forceinline__ unsigned cvtpk(float lo, float hi) {
    typedef __bf16 bf16x2_t __attribute__((ext_vector_type(2)));
    return __builtin_bit_cast(unsigned, __builtin_convertvector((f32x2){lo, hi}, bf16x2_t)); }
__device__ __forceinline__ float bfr(float f) { return __uint_as_float(cvtpk(f, 0.f) << 16); }

#define DSC " sc1"
__device__ __forceinline__ void glds16d(unsigned voff, const void* sbase, unsigned ldsdst) {
    asm volatile("s_mov_b32 m0, %2\n\ts_nop 0\n\tglobal_load_lds_dwordx4 %0, %1" DSC :: "v"(voff), "s"(sbase), "s"(ldsdst) : "memory");
}
__device__ __forceinline__ void glds2d(unsigned v0, unsigned v1, const void* sbase, unsigned ldsdst) {
    asm volatile("s_mov_b32 m0, %3\n\ts_nop 0\n\tglobal_load_lds_dwordx4 %0, %2" DSC "\n\tglobal_load_lds_dwordx4 %1, %2 offset:1024" DSC
                 :: "v"(v0), "v"(v1), "s"(sbase), "s"(ldsdst) : "memory");
}
__device__ __forceinline__ void glds4d(unsigned v0, unsigned v1, unsigned v2, unsigned v3, const void* sbase, unsigned ldsdst) {
    asm volatile("s_mov_b32 m0, %5\n\ts_nop 0\n\tglobal_load_lds_dwordx4 %0, %4" DSC "\n\tglobal_load_lds_dwordx4 %1, %4 offset:1024" DSC "\n\t"
                 "global_load_lds_dwordx4 %2, %4 offset:2048" DSC "\n\tglobal_load_lds_dwordx4 %3, %4 offset:3072" DSC
                 :: "v"(v0), "v"(v1), "v"(v2), "v"(v3), "s"(sbase), "s"(ldsdst) : "memory");
}
__device__ __forceinline__ void glds2(unsigned v0, unsigned v1, const void* sbase, unsigned ldsdst) {
    asm volatile("s_mov_b32 m0, %3\n\ts_nop 0\n\tglobal_load_lds_dwordx4 %0, %2\n\tglobal_load_lds_dwordx4 %1, %2 offset:1024"
                 :: "v"(v0), "v"(v1), "s"(sbase), "s"(ldsdst) : "memory");
}
__device__ __forceinline__ void glds4(unsigned v0, unsigned v1, unsigned v2, unsigned v3, const void* sbase, unsigned ldsdst) {
    asm volatile("s_mov_b32 m0, %5\n\ts_nop 0\n\tglobal_load_lds_dwordx4 %0, %4\n\tglobal_load_lds_dwordx4 %1, %4 offset:1024\n\t"
                 "global_load_lds_dwordx4 %2, %4 offset:2048\n\tglobal_load_lds_dwordx4 %3, %4 offset:3072"
                 :: "v"(v0), "v"(v1), "v"(v2), "v"(v3), "s"(sbase), "s"(ldsdst) : "memory");
}
template <int N> __device__ __forceinline__ void wait_bar() {
    SBAR();
    asm volatile("s_waitcnt vmcnt(%0)\n\ts_barrier" :: "n"(N) : "memory");
    SBAR();
}
__device__ __forceinline__ void bar() { SBAR(); asm volatile("s_barrier" ::: "memory"); SBAR(); }
__device__ __forceinline__ void bar0() { SBAR(); asm volatile("s_waitcnt lgkmcnt(0)\n\ts_barrier" ::: "memory"); SBAR(); }
__device__ __forceinline__ bf16x8 ld128(lds_cptr p) { return *(const LAS bf16x8*)p; }
__device__ __forceinline__ bf16x8 ldtr(lds_cptr p) {
    s16x4 lo = __builtin_amdgcn_ds_read_tr16_b64_v4i16((LAS s16x4*)p);
    s16x4 hi = __builtin_amdgcn_ds_read_tr16_b64_v4i16((LAS s16x4*)(p + 512));
    return (bf16x8){lo[0], lo[1], lo[2], lo[3], hi[0], hi[1], hi[2], hi[3]};
}
__device__ __forceinline__ u32x4 pack8(const f32x16& a, int s, float sc) {
    u32x4 w;
    w[0] = cvtpk(a[8 * s + 0] * sc, a[8 * s + 1] * sc); w[1] = cvtpk(a[8 * s + 2] * sc, a[8 * s + 3] * sc);
    w[2] = cvtpk(a[8 * s + 4] * sc, a[8 * s + 5] * sc); w[3] = cvtpk(a[8 * s + 6] * sc, a[8 * s + 7] * sc);
    return w;
}
#define STF " sc1"
#define ST16(p, v) do { const u32x4 v_ = (v); asm volatile("global_store_dwordx4 %0, %1, off" STF "\n\ts_nop 1" :: "v"(p), "v"(v_) : "memory"); } while (0)
__device__ __forceinline__ int swap23(int i) { return (i & ~12) | ((i & 4) << 1) | ((i & 8) >> 1); }

__device__ __forceinline__ int lf_order(int tau, int a, int c, int G) {
    if (tau == 0) return a;
    const int g = (tau - 1) >> 2, pos = (tau - 1) & 3;
    if (g != G - 1 && pos == c) return 4 * (g + 1) + a;
    const int b = (g == G - 1 || pos < c) ? pos : pos - 1;
    return 4 * g + (b < a ? b : b + 1);
}

__device__ __forceinline__ int rswz(int row) { return ((row >> 1) & 1) | (((row >> 3) & 3) << 1); }
__device__ __forceinline__ u32x4 pack44(const f32x4& a, const f32x4& b, float sc) {
    return (u32x4){cvtpk(a[0] * sc, a[1] * sc), cvtpk(a[2] * sc, a[3] * sc), cvtpk(b[0] * sc, b[1] * sc), cvtpk(b[2] * sc, b[3] * sc)};
}
__device__ __forceinline__ void drain() { asm volatile("s_waitcnt vmcnt(0)" ::: "memory"); }
typedef unsigned long long u64;
__device__ __forceinline__ u64 ld_tag(const char* p) { return __hip_atomic_load((const u64*)p, __ATOMIC_RELAXED, __HIP_MEMORY_SCOPE_AGENT); }
__device__ __forceinline__ void st_tag(char* p, u64 v) { __hip_atomic_store((u64*)p, v, __ATOMIC_RELAXED, __HIP_MEMORY_SCOPE_AGENT); }
__device__ __forceinline__ void tags_acquire(const char* p, u64 v, u64 tag) {
#pragma nounroll
    for (int spin = 0; !__all(v == tag) && spin < (1 << 22); ++spin) { __builtin_amdgcn_s_sleep(2); v = ld_tag(p); }
    asm volatile("" ::: "memory");
}
__device__ __forceinline__ u64 call_tag(const char* ws) {
    const u64 e = ld_tag(ws + WS_E) + 1;
    return ((u64)(unsigned)__builtin_amdgcn_readfirstlane((int)(e >> 32)) << 32) | (unsigned)__builtin_amdgcn_readfirstlane((int)e);
}
__device__ __forceinline__ int fresh_lane() {
    int l; asm volatile("v_mbcnt_lo_u32_b32 %0, -1, 0\n\tv_mbcnt_hi_u32_b32 %0, -1, %0" : "=v"(l)); return l;
}
__device__ __forceinline__ float DOT2(unsigned a, unsigned b, float acc) {
    typedef __bf16 bf2_t __attribute__((ext_vector_type(2)));
    return __builtin_amdgcn_fdot2_f32_bf16(__builtin_bit_cast(bf2_t, a), __builtin_bit_cast(bf2_t, b), acc, false);
}
__device__ __forceinline__ void bars(int n) { for (int i = 0; i < n; ++i) bar(); }

constexpr int M_KS = 8192, M_G0 = 4 * M_KS, M_GS = 32768, M_H = 16384, M_LDS = M_G0 + 4 * M_GS;
constexpr int CODE_KB = 23;
constexpr int NA = 32, NY = 44, NO = 76;
__global__ __launch_bounds__(1024, 4) void k_all(const u16* __restrict__ x, const u16* __restrict__ cr, const u16* __restrict__ ci,
                                                  const u16* __restrict__ hr, const u16* __restrict__ hi, const u16* __restrict__ wqkv,
                                                  const u16* __restrict__ wo, const u16* __restrict__ gw, char* ws, u16* __restrict__ out) {
    extern __shared__ __attribute__((aligned(16))) char smem[];
    LAS char* lds = (LAS char*)smem;
    unsigned long long pc0; asm volatile("s_getpc_b64 %0" : "=s"(pc0));
    const int w = (__builtin_amdgcn_readfirstlane(threadIdx.x >> 6) + 12) & 15;
    const int xcd = blockIdx.x & 7, idx = blockIdx.x >> 3;
    const int rt = (xcd & 1) * 8 + (idx & 7), j = (xcd >> 1) * 4 + (idx >> 3);
    if (w >= 12) {
        const int l = fresh_lane(), g = w - 12, k0 = ((idx >> 3) - (idx & 7)) & 3, r8 = l >> 3;
        const unsigned d = (unsigned)(uintptr_t)lds;
        const unsigned v0 = (unsigned)(r8 * 2048 + (((l & 7) ^ ((r8 >> 1) & 1)) << 4));
        const char* xb = (const char*)x + (size_t)((rt * 128 + g * 32) * 2048 + k0 * 128);
        glds4(v0, (v0 ^ 32) + 15360, (v0 ^ 64) + 30720, (v0 ^ 96) + 46080, xb, d + M_G0 + g * 4096);
        const unsigned w0 = (unsigned)(r8 * 6144 + (((l & 7) ^ (((l >> 4) & 1) << 2)) << 4)), w1 = (w0 ^ 32) + 48128;
        const char* b = (const char*)wqkv + (size_t)((k0 * 64 + g * 16) * 6144 + j * 128);
        glds2(w0, w1, b, d + M_G0 + M_H + g * 2048); glds2(w0, w1, b + 2048, d + M_G0 + M_H + 8192 + g * 2048); glds2(w0, w1, b + 4096, d + g * 2048);
    } else if ((idx & 1) * 12 + w < CODE_KB) {
        u32x4 v; asm volatile("global_load_dwordx4 %0, %1, %2\n\ts_waitcnt vmcnt(0)" : "=&v"(v) : "v"(((idx & 1) * 12 + w) * 1024 + fresh_lane() * 16), "s"(pc0 & ~255ull) : "memory");
    }
    const int head = xcd * 4 + (idx >> 3), qb = idx & 7;
    const int part = xcd >> 2, tt = ((xcd >> 1) & 1) * 8 + (idx & 7), et = (xcd & 1) * 4 + (idx >> 3);
    const char* kb = ws + WS_KV + (size_t)head * 131072;
#define LANE_VARS() const int lane = fresh_lane(), r = lane & 31, hh = lane >> 5, c = lane & 15, g4 = lane >> 4, sg0 = 8 * (c >> 2) + (c & 3); \
    lds_cptr lp = (lds_cptr)lds + lane * 16; (void)r; (void)hh; (void)g4; (void)sg0; (void)lp
    char* myflag = ws + WS_F + blockIdx.x * 8;
    if (w >= 12) {
        LANE_VARS();
        const int g = w - 12;
        const int la = ((idx >> 3) - (idx & 7)) & 3, lc = (idx >> 1) & 3;
        const int tab16 = lf_order(lane & 15, la, lc, 4), tab32 = lf_order(lane & 31, la, lc, 8);
        const unsigned lds0 = (unsigned)(uintptr_t)lds;
        const unsigned vL = (unsigned)(lane * 16);
        unsigned vR[4], vW[2], vC[4], vA[2], vT[2];
#pragma unroll
        for (int i = 0; i < 4; ++i) {
            vR[i] = (unsigned)((8 * i + (lane >> 3)) * 2048 + (((lane & 7) ^ rswz(8 * i + (lane >> 3))) << 4) - i * 1024);
            vC[i] = (unsigned)((8 * i + (lane >> 3)) * 4096 + (((lane & 7) ^ ((4 * i + (lane >> 4)) & 7)) << 4) - i * 1024);
        }
#pragma unroll
        for (int i = 0; i < 2; ++i) {
            vW[i] = (unsigned)((8 * i + (lane >> 3)) * 6144 + (((lane & 7) ^ (((lane >> 4) & 1) << 2) ^ (i << 1)) << 4) - i * 1024);
            vT[i] = (unsigned)((8 * i + (lane >> 3)) * 2048 + (((lane & 7) ^ (((lane >> 4) & 1) << 2)) << 4) - i * 1024);
            vA[i] = (unsigned)(lane * 16 + i * (65536 - 2048));
        }
        const unsigned vKV = (unsigned)(lane * 16 + (4 << 20) - 3072 - 1024);
        const char* xb = (const char*)x + (size_t)(rt * 128 + g * 32) * 2048;
        const char* hb = (const char*)(j < 8 ? hr : hi) + (size_t)((j & 7) * 128 + g * 32) * 2048;
        const char* wb = (const char*)wqkv + (size_t)g * 16 * 6144 + j * 128;
        const char* cb = (const char*)(part ? ci : cr) + (size_t)(tt * 128 + 32 * g) * 4096;
        const char* ab = ws + WS_AB + (size_t)(part * 64 + et * 8 + 2 * g) * 65536;
        const char* ob = ws + WS_O + (size_t)(tt * 4 + g) * 65536;
        const char* wob = (const char*)wo + (2 * et + part) * 128;
        const unsigned dG = lds0 + M_G0 + g * 4096, dK = lds0 + g * 2048;
        auto sP = [&](int n) {
            const int kp = __builtin_amdgcn_readlane(tab16, n); const unsigned so = (n & 3) * M_GS;
            const char* b = wb + (size_t)(kp * (64 * 6144));
            glds4(vR[0], vR[1], vR[2], vR[3], xb + kp * 128, dG + so);
            glds2(vW[0], vW[1], b, dG + so + M_H - g * 2048); glds2(vW[0], vW[1], b + 2048, dG + so + M_H + 8192 - g * 2048);
            glds2(vW[0], vW[1], b + 4096, dK + (n & 3) * M_KS);
        };
        auto sH = [&](int n) {
            const int kp = __builtin_amdgcn_readlane(tab16, n & 15); const unsigned so = (n & 3) * M_GS;
            glds4(vR[0], vR[1], vR[2], vR[3], xb + kp * 128, dG + so); glds4(vR[0], vR[1], vR[2], vR[3], hb + kp * 128, dG + so + M_H);
        };
        auto sKV = [&](int n) {
            glds2d(vL, vKV, kb + ((n - NA) * 4096 + 4096 + g * 1024), dK + (n & 3) * M_KS);
        };
        auto sY = [&](int n) {
            const int kp = __builtin_amdgcn_readlane(tab32, n - NY); const unsigned so = (n & 3) * M_GS;
            glds4(vC[0], vC[1], vC[2], vC[3], cb + kp * 128, dG + so); glds4d(vA[0], vA[0], vA[1], vA[1], ab + kp * 2048, dG + so + M_H);
        };
        auto sO = [&](int n) {
            const int kp = __builtin_amdgcn_readlane(tab16, n - NO); const unsigned so = (n & 3) * M_GS;
            glds2(vT[0], vT[1], wob + (size_t)((kp * 64 + g * 16) * 2048), dG + so + M_H - g * 2048);
            glds4d(vL, vL, vL, vL, ob + kp * 4096, dG + so);
        };
#define WB(N) wait_bar<N>()
        auto sC = [&](int n) { const int kp = __builtin_amdgcn_readlane(tab32, n - NY); glds4(vC[0], vC[1], vC[2], vC[3], cb + kp * 128, dG + (n & 3) * M_GS); };
        auto sAB = [&](int n) { const int kp = __builtin_amdgcn_readlane(tab32, n - NY); glds4d(vA[0], vA[0], vA[1], vA[1], ab + kp * 2048, dG + (n & 3) * M_GS + M_H); };
        sP(1); WB(10);
        sP(2);
        for (int k = 1; k < 14; ++k) { sP(k + 2); WB(20); }
        sH(16); WB(18);
        for (int k = 15; k < 30; ++k) { sH(k + 2); WB(16); }
        glds16d(vL, kb + g * 1024, dK + 3 * M_KS); glds16d(vL, kb + (4 << 20), dK + 3 * M_KS + 1024);
        WB(10);
        for (int k = 31; k < 36; ++k) { sKV(k + 1); WB(2); }
        sKV(37); sC(44); WB(6);
        for (int k = 37; k < 40; ++k) { sKV(k + 1); sC(k + 8); WB(10); }
        sKV(41); WB(6);
        sKV(42); WB(2);
        sKV(43); WB(2);
        sKV(44); sAB(44); sAB(45); WB(10);
        sKV(45); sAB(46); WB(10);
        sKV(46); sAB(47); WB(10);
        sKV(47); sY(48); WB(14);
        for (int k = 47; k < 62; ++k) { sKV(k + 1); sY(k + 2); WB(18); }
        { const int vo = g < 3 ? 31 * 4096 + 1024 + g * 1024 : 0;
          glds16d(vL, kb + (4 << 20) + vo, dK + 3 * M_KS + 1024); sY(64); WB(17); }
        for (int k = 63; k < 70; ++k) { sY(k + 2); WB(16); }
        glds16d(vL, ws + WS_G + (size_t)tt * 1024, lds0 + 16384);
        sY(72); WB(17);
        sY(73); WB(17);
        sY(74); WB(16);
        sY(75); WB(16);
        WB(8);
        sO(76);
        for (int k = 75; k < 89; ++k) { sO(k + 2); WB(12); }
        sO(91); glds4d(vL, vL, vL, vL, ws + WS_Y + (size_t)(blockIdx.x ^ 4) * 16384 + g * 4096, lds0 + 16384 + g * 4096);
        WB(16);
        WB(10);
        WB(0);
        static_assert(NA == 32 && NY == 44 && NO == 76, "the DMA program is written for this schedule");
    } else if (w >= 8) {
        LANE_VARS();
        const int g = w - 8, gm = g >> 1, gn = g & 1;
        const u64 tag = call_tag(ws);
        __builtin_amdgcn_s_setprio(3);
        bars(2);
        {
            const u32x4* xr = (const u32x4*)(x + (size_t)(blockIdx.x * 8 + 2 * g) * 1024); const u32x4* gr = (const u32x4*)gw;
            const u32x4 a0 = xr[lane], a1 = xr[lane + 64], a2 = xr[lane + 128], a3 = xr[lane + 192], b0 = gr[lane], b1 = gr[lane + 64];
            bars(4);
            float z0 = 0.f, z1 = 0.f;
#pragma unroll
            for (int e = 0; e < 4; ++e) { z0 = DOT2(a0[e], b0[e], z0); z1 = DOT2(a2[e], b0[e], z1); z0 = DOT2(a1[e], b1[e], z0); z1 = DOT2(a3[e], b1[e], z1); }
#define DPP_ADD(v, ctrl, rmask) v += __uint_as_float(__builtin_amdgcn_update_dpp(0u, __float_as_uint(v), ctrl, rmask, 0xf, false))
            DPP_ADD(z0, 0x111, 0xf); DPP_ADD(z1, 0x111, 0xf); DPP_ADD(z0, 0x112, 0xf); DPP_ADD(z1, 0x112, 0xf);
            DPP_ADD(z0, 0x114, 0xf); DPP_ADD(z1, 0x114, 0xf); DPP_ADD(z0, 0x118, 0xf); DPP_ADD(z1, 0x118, 0xf);
            DPP_ADD(z0, 0x142, 0xa); DPP_ADD(z1, 0x142, 0xa); DPP_ADD(z0, 0x143, 0xc); DPP_ADD(z1, 0x143, 0xc);
            z0 = __uint_as_float(__builtin_amdgcn_readlane(__float_as_uint(z0), 63)); z1 = __uint_as_float(__builtin_amdgcn_readlane(__float_as_uint(z1), 63));
            bar();
            const float gt = bfr(1.f / (1.f + __expf(-bfr((lane & 1) ? z1 : z0))));
            if (lane < 2) st_tag(ws + WS_G + (size_t)(blockIdx.x * 8 + 2 * g + lane) * 8, ((u64)__float_as_uint(bfr(1.f - gt)) << 32) | __float_as_uint(gt));
            bars(6);
            __builtin_amdgcn_s_setprio(0);
            bars(2);
        }
        f32x4 y[4][4];
        bf16x8 cA[4], cB[4], fa[4];
        lds_cptr ca[2][2], fp[2][2];
#define G_MM(SW, a_, CX, m_) y[a_][m_] = (SW) ? MFMA16(CX[m_], fa[a_], y[a_][m_]) : MFMA16(fa[a_], CX[m_], y[a_][m_])
#define G_STEP(SW, CP, CC, ST, HF, CO, FO) do { \
            _Pragma("unroll") for (int s_ = 0; s_ < 4; ++s_) { \
                _Pragma("unroll") for (int m = 0; m < 4; ++m) { \
                    if (s_ < 2) G_MM(SW, s_ + 2, CP, m); else G_MM(SW, s_ - 2, CC, m); \
                    if (s_ == 0) CC[m] = ld128(ca[(ST) >> 1][HF] + ((ST) & 1) * M_GS + CO(m)); \
                    if (m == 0 && s_ < 3) fa[s_] = ld128(fp[(ST) >> 1][HF] + ((ST) & 1) * M_GS + FO(s_)); \
                    if (m == 2 && s_ == 2) fa[3] = ld128(fp[(ST) >> 1][HF] + ((ST) & 1) * M_GS + FO(3)); \
                    SBAR(); } } } while (0)
#define G_ITER(SW, st, CO, FO) do { bar0(); G_STEP(SW, cB, cA, st, 0, CO, FO); G_STEP(SW, cA, cB, st, 1, CO, FO); } while (0)
#define G_RUN(SW, NIT, CO, FO, PRE) do { \
            _Pragma("unroll") for (int a = 0; a < 4; ++a) { cB[a] = (bf16x8){}; fa[a] = (bf16x8){}; \
                _Pragma("unroll") for (int b = 0; b < 4; ++b) y[a][b] = (f32x4){}; } \
            PRE; LGKM0(); \
            for (int it = 0; it < NIT; ++it) { G_ITER(SW, 0, CO, FO); G_ITER(SW, 1, CO, FO); G_ITER(SW, 2, CO, FO); G_ITER(SW, 3, CO, FO); } \
            bar0(); \
            _Pragma("unroll") for (int a = 2; a < 4; ++a) _Pragma("unroll") for (int m = 0; m < 4; ++m) G_MM(SW, a, cB, m); } while (0)
#define OFF_SIG(i) (((i) >> 1) * 4096 + ((i) & 1) * 512)
#define OFF_2K(i) ((i) * 2048)
#pragma unroll
        for (int h = 0; h < 2; ++h)
#pragma unroll
            for (int hf = 0; hf < 2; ++hf) {
                const int ra = M_G0 + h * 2 * M_GS + sg0 * 128 + (((4 * hf + g4) ^ rswz(sg0)) << 4);
                ca[h][hf] = (lds_cptr)lds + ra + gm * 8192; fp[h][hf] = (lds_cptr)lds + ra + M_H + gn * 8192; PIN(ca[h][hf]); PIN(fp[h][hf]);
            }
        G_RUN(true, 4, OFF_SIG, OFF_SIG, drain(); bar());
      { LANE_VARS();
#pragma unroll
        for (int gr = 0; gr < 2; ++gr) {
            char* abp = ws + WS_AB + ((size_t)(j * 8 + gn * 4) * 64 + rt * 4 + gm * 2 + gr) * 1024 + lane * 16;
#pragma unroll
            for (int b = 0; b < 4; ++b) ST16(abp + (size_t)b * 65536, pack44(y[b][2 * gr], y[b][2 * gr + 1], 1.f));
            bar();
        }
        drain();
        bar();
        if (g == 0) {
            if (lane == 0) st_tag(myflag + 2048, tag);
            const int jj = part * 8 + et, rr = lane & 15;
            const char* fpn = ws + WS_F + 2048 + (((rr & 7) | ((jj & 3) << 3)) * 8 + ((rr >> 3) | ((jj >> 2) << 1))) * 8;
            bars(5);
            const u64 v = ld_tag(fpn);
            bar();
            tags_acquire(fpn, v, tag);
        } else bars(6);
        bar();
#pragma unroll
        for (int h = 0; h < 2; ++h)
#pragma unroll
            for (int hf = 0; hf < 2; ++hf) {
                ca[h][hf] = (lds_cptr)lds + M_G0 + h * 2 * M_GS + (gm * 64 + c) * 128 + (((4 * hf + g4) ^ ((c >> 1) & 7)) << 4);
                fp[h][hf] = lp + M_G0 + h * 2 * M_GS + M_H + gn * 8192 + hf * 1024; PIN(ca[h][hf]); PIN(fp[h][hf]);
            }
      }
        G_RUN(false, 8, OFF_2K, OFF_2K, bar());
      { LANE_VARS();
        __builtin_amdgcn_s_setprio(3);
#pragma unroll
        for (int m = 0; m < 4; ++m) {
            float gy = *(const LAS float*)(lds + 16384 + (gm * 64 + m * 16 + c) * 8);
            if (part) gy = -gy;
#pragma unroll
            for (int e = 0; e < 2; ++e) {
                const int off = ((gm * 2 + (m >> 1)) * 2 + e) * 2048 + (g4 >> 1) * 1024 + ((g4 & 1) * 32 + (m & 1) * 16 + c) * 16;
                const u32x4 v = pack44(y[2 * e][m], y[2 * e + 1][m], gy);
                if (gn == part) *(LAS u32x4*)(lds + off) = v; else ST16(ws + WS_Y + (size_t)blockIdx.x * 16384 + off, v);
            }
            bar();
        }
        bar();
        asm volatile("s_waitcnt vmcnt(0) lgkmcnt(0)" ::: "memory");
        bar();
        if (g == 0) {
            if (lane == 0) st_tag(myflag + 6144, tag);
            const char* fpn = ws + WS_F + 6144 + (blockIdx.x ^ 4) * 8;
            bars(4);
            const u64 v = ld_tag(fpn);
            bar();
            tags_acquire(fpn, v, tag);
            bar();
        }
      }
        __builtin_amdgcn_s_waitcnt(0x0F70);
    } else {
        LANE_VARS();
        const u64 tag = call_tag(ws);
        {
            const int wm = w >> 2, wn = (w >> 1) & 1, wq = w & 1;
            lds_cptr xa[2][2], tq[2][2], tv;
            { const int q4 = c >> 2, p4 = c & 3;
#pragma unroll
              for (int i = 0; i < 2; ++i) {
                  const int tbo = (8 * g4 + q4) * 128 + ((((wn * 4 + i * 2) ^ ((q4 >> 1) << 2) ^ ((g4 & 1) << 1)) + (p4 >> 1)) << 4) + (p4 & 1) * 8;
#pragma unroll
                  for (int h = 0; h < 2; ++h) {
                      xa[h][i] = (lds_cptr)lds + M_G0 + h * 2 * M_GS + wm * 8192 + sg0 * 128 + (((4 * i + g4) ^ rswz(sg0)) << 4); PIN(xa[h][i]);
                      tq[h][i] = (lds_cptr)lds + M_G0 + h * 2 * M_GS + M_H + wq * 8192 + tbo; PIN(tq[h][i]);
                  } }
              tv = (lds_cptr)lds + (8 * g4 + q4) * 128 + ((((wn * 4 + wq * 2) ^ ((q4 >> 1) << 2) ^ ((g4 & 1) << 1)) + (p4 >> 1)) << 4) + (p4 & 1) * 8; PIN(tv); }
            f32x4 acc[3][4];
            bf16x8 xA[4], xB[4], fb[3];
#pragma unroll
            for (int m = 0; m < 4; ++m) { acc[0][m] = (f32x4){}; acc[1][m] = (f32x4){}; acc[2][m] = (f32x4){}; xB[m] = (bf16x8){}; }
            fb[1] = fb[2] = (bf16x8){};
#define Q_MM(b_, XF, m_) acc[b_][m_] = (b_) < 2 ? MFMA16(fb[b_], XF[m_], acc[b_][m_]) : MFMA16(XF[m_], fb[b_], acc[b_][m_])
#define Q_STEP(XP, XC, ST, HF) do { \
                _Pragma("unroll") for (int s_ = 0; s_ < 3; ++s_) { \
                    _Pragma("unroll") for (int m = 0; m < 4; ++m) { \
                        if (s_ < 2) Q_MM(s_ + 1, XP, m); else Q_MM(0, XC, m); \
                        SBAR(); \
                        if (s_ < 2 && m >= 2) XC[2 * s_ + m - 2] = ld128(xa[(ST) >> 1][HF] + ((ST) & 1) * M_GS + s_ * 4096 + (m - 2) * 512); \
                        if (m == 0) fb[s_] = ldtr((HF) * 4096 + (s_ < 2 ? tq[(ST) >> 1][s_ & 1] + ((ST) & 1) * M_GS : tv + (ST) * M_KS)); \
                        SBAR(); } } } while (0)
#define Q_ITER(st) do { bar0(); Q_STEP(xB, xA, st, 0); Q_STEP(xA, xB, st, 1); } while (0)
            LGKM0();
            for (int it = 0; it < 4; ++it) { Q_ITER(0); Q_ITER(1); Q_ITER(2); Q_ITER(3); }
            bar0();
#pragma unroll
            for (int b = 1; b < 3; ++b)
#pragma unroll
                for (int m = 0; m < 4; ++m) Q_MM(b, xB, m);
            const int hd = 2 * j + wn;
            const float qs = wq ? 1.f : 0.17677669529663687f * 1.4426950408889634f;
            char* bp = ws + (size_t)(hd * 64 + rt * 4 + wm * 2) * 2048 + (g4 >> 1) * 1024 + (g4 & 1) * 512;
            char* qk = bp + (wq ? WS_KV + (16 * (c >> 3) + (c & 7)) * 16 : WS_Q + sg0 * 16);
            const int as = wq ? 128 : 64;
#pragma unroll
            for (int gr = 0; gr < 2; ++gr) { ST16(bp + WS_KV + (4u << 20) + gr * 2048 + (16 * wq + swap23(c)) * 16, pack44(acc[2][2 * gr], acc[2][2 * gr + 1], 1.f)); }
            bar();
#pragma unroll
            for (int a = 0; a < 2; ++a) ST16(qk + a * as, pack44(acc[0][a], acc[1][a], qs));
            bar();
#pragma unroll
            for (int a = 0; a < 2; ++a) ST16(qk + 2048 + a * as, pack44(acc[0][2 + a], acc[1][2 + a], qs));
        }
        bars(QD);
        drain();
        bar();
        if (w == 0) {
            if (lane == 0) st_tag(myflag, tag);
            const int jj = head >> 1, rr = lane & 15;
            const int pb = lane < 16 ? ((rr & 7) | ((jj & 3) << 3)) * 8 + ((rr >> 3) | ((jj >> 2) << 1)) : (lane < 32 ? tt * 16 + rr : qb * 32 + (lane - 32));
            const char* fpn = ws + WS_F + pb * 8;
            bars(8 - QD);
            const u64 v = ld_tag(fpn);
            bar();
            tags_acquire(fpn, v, tag);
        } else bars(9 - QD);
        bar();
        const int qt = qb * 8 + w;
        bf16x8 qf[2]; float omg;
        { const char* qp = ws + WS_Q + (size_t)(head * 64 + qt) * 2048 + lane * 16;
          asm volatile("global_load_dwordx4 %0, %1, off sc1" : "=v"(qf[0]) : "v"(qp) : "memory");
          asm volatile("global_load_dwordx4 %0, %1, off offset:1024 sc1" : "=v"(qf[1]) : "v"(qp) : "memory");
          asm volatile("global_load_dword %0, %1, off offset:4 sc1" : "=v"(omg) : "v"(ws + WS_G + (size_t)(qt * 32 + r) * 8) : "memory"); }
        bf16x8 sel; { const short one = (((lane & 1) == ((lane >> 4) & 1))) ? (short)0x3f80 : (short)0;
#pragma unroll
            for (int i = 0; i < 8; ++i) sel[i] = one; }
        f32x16 o = (f32x16){}; f32x4 l4 = (f32x4){};
        f32x16 negm, sA, sB;
        bf16x8 kf[2], vf0, vf1;
        u32x4 p0, p1;
        bars(2);
        asm volatile("s_waitcnt vmcnt(0)" : "+v"(qf[0]), "+v"(qf[1]), "+v"(omg) :: "memory");
        kf[0] = ld128(lp + 3 * M_KS); kf[1] = ld128(lp + 3 * M_KS + 2048);
        sA = MFMA32(kf[0], qf[0], (f32x16){}); sA = MFMA32(kf[1], qf[1], sA);
        { float m = sA[0];
#pragma unroll
          for (int i = 1; i < 16; ++i) m = fmaxf(m, sA[i]);
          m = fmaxf(m, __shfl_xor(m, 32));
#pragma unroll
          for (int i = 0; i < 16; ++i) { negm[i] = -m; sA[i] -= m; } }
        kf[0] = ld128(lp + 3 * M_KS + 4096); kf[1] = ld128(lp + 3 * M_KS + 6144);
        vf0 = ld128(lp + 3 * M_KS + 1024);
        vf1 = vf0; p1 = (u32x4){0u, 0u, 0u, 0u};
#define EN(S, i, n) do { _Pragma("unroll") for (int e_ = 0; e_ < n; ++e_) S[i + e_] = EX2(S[i + e_]); } while (0)
#define A_STEP(SC, SN, v1o, kn, v0n) do { \
            SN = MFMA32(kf[0], qf[0], negm); PIN(SN); SBAR(); \
            EN(SC, 0, 3); PIN(SC); SBAR(); \
            o = MFMA32(vf1, PB(p1), o); PIN(o); SBAR(); \
            EN(SC, 3, 3); PIN(SC); SBAR(); \
            SN = MFMA32(kf[1], qf[1], SN); PIN(SN); SBAR(); \
            vf1 = ld128(lp + (v1o)); kf[0] = ld128(lp + (kn)); kf[1] = ld128(lp + (kn) + 2048); SBAR(); \
            EN(SC, 6, 2); p0[0] = cvtpk(SC[0], SC[1]); p0[1] = cvtpk(SC[2], SC[3]); PIN(SC); PIN(p0); SBAR(); \
            l4 = MFMA16(sel, PB(p1), l4); PIN(l4); SBAR(); \
            p0[2] = cvtpk(SC[4], SC[5]); p0[3] = cvtpk(SC[6], SC[7]); PIN(p0); SBAR(); EN(SC, 8, 2); PIN(SC); SBAR(); \
            o = MFMA32(vf0, PB(p0), o); PIN(o); SBAR(); \
            vf0 = ld128(lp + (v0n)); SBAR(); \
            EN(SC, 10, 3); PIN(SC); SBAR(); \
            l4 = MFMA16(sel, PB(p0), l4); PIN(l4); SBAR(); \
            EN(SC, 13, 3); \
            _Pragma("unroll") for (int i = 0; i < 4; ++i) p1[i] = cvtpk(SC[8 + 2 * i], SC[9 + 2 * i]); \
            PIN(p1); SBAR(); \
        } while (0)
#define A_ITER(st) do { \
            constexpr int b0 = (st) * M_KS; \
            bar(); \
            A_STEP(sA, sB, b0 + 1024, b0, b0 + 3072); \
            A_STEP(sB, sA, b0 + 5120, b0 + 4096, b0 + 7168); \
            } while (0)
        static_assert(NA % 4 == 0 && NY % 4 == 0 && NO % 4 == 0, "slot phase");
        LGKM0();
        for (int it = 0; it < 8; ++it) { A_ITER(0); A_ITER(1); A_ITER(2); A_ITER(3); }
        bar();
        o = MFMA32(vf1, PB(p1), o); l4 = MFMA16(sel, PB(p1), l4);
        float l = ((lane >> 4) & 1) ? l4[1] : l4[0];
        bool ok = l < 1.0e30f;
#pragma unroll
        for (int i = 0; i < 16; ++i) ok = ok && (fabsf(o[i]) < 3.0e38f);
        if (!__all(ok)) {
            float m = -INFINITY, ls = 0.f; o = (f32x16){};
            const char* gp = kb + lane * 16;
            for (int t = 0; t < 64; ++t) {
                bf16x8 k0, k1, v0, v1;
                asm volatile("global_load_dwordx4 %0, %4, off sc1\n\tglobal_load_dwordx4 %1, %4, off offset:1024 sc1\n\t"
                             "global_load_dwordx4 %2, %5, off sc1\n\tglobal_load_dwordx4 %3, %5, off offset:1024 sc1\n\ts_waitcnt vmcnt(0)"
                             : "=&v"(k0), "=&v"(k1), "=&v"(v0), "=&v"(v1) : "v"(gp + t * 2048), "v"(gp + (4 << 20) + t * 2048) : "memory");
                f32x16 s = MFMA32(k0, qf[0], (f32x16){}); s = MFMA32(k1, qf[1], s);
                float tm = s[0];
#pragma unroll
                for (int i = 1; i < 16; ++i) tm = fmaxf(tm, s[i]);
                tm = fmaxf(tm, __shfl_xor(tm, 32));
                const float mn = fmaxf(m, tm), al = EX2(m - mn);
                m = mn; ls *= al;
#pragma unroll
                for (int i = 0; i < 16; ++i) { o[i] *= al; s[i] = EX2(s[i] - mn); ls += s[i]; }
                u32x4 a0, a1;
#pragma unroll
                for (int i = 0; i < 4; ++i) { a0[i] = cvtpk(s[2 * i], s[2 * i + 1]); a1[i] = cvtpk(s[8 + 2 * i], s[9 + 2 * i]); }
                o = MFMA32(v0, PB(a0), o); o = MFMA32(v1, PB(a1), o);
            }
            l = ls + __shfl_xor(ls, 32);
        }
        { const float sc = omg / l;
          char* op = ws + WS_O + ((size_t)qt * 64 + 2 * head) * 1024 + lane * 16;
#pragma unroll
          for (int s = 0; s < 2; ++s) ST16(op + s * 1024, pack8(o, s, sc)); }
        bar();
        drain();
        bar();
        if (w == 0) {
            if (lane == 0) st_tag(myflag + 4096, tag);
            const int hd = lane & 31;
            const char* fpn = ws + WS_F + 4096 + (((hd & 3) * 8 + (tt >> 1)) * 8 + (hd >> 2)) * 8;
            bars(6);
            const u64 v = ld_tag(fpn);
            bar();
            tags_acquire(fpn, v, tag);
        } else bars(7);
        bars(2);
        {
            const int wm = w >> 1, wn = w & 1;
            const int q4 = (lane & 15) >> 2, p4 = lane & 3, cg = (lane >> 4) & 1, pi4 = ((p4 & 1) << 1) | (p4 >> 1);
            lds_cptr ap[2], bp[2];
#pragma unroll
            for (int h = 0; h < 2; ++h) {
                ap[h] = lp + M_G0 + h * 2 * M_GS + wm * 4096;
                bp[h] = (lds_cptr)lds + M_G0 + h * 2 * M_GS + M_H + (8 * hh + q4) * 128 + ((wn ^ (q4 >> 1)) << 6) + cg * 32 + pi4 * 8;
            }
            f32x16 acc = (f32x16){};
            bf16x8 a0[4], b0[4], a1[4], b1[4];
#pragma unroll
            for (int i = 0; i < 4; ++i) { a1[i] = (bf16x8){}; b1[i] = (bf16x8){}; }
#define F_ITER(st, AP, BP, AC, BC) do { \
                bar0(); \
                _Pragma("unroll") for (int i = 0; i < 4; ++i) { \
                    AC[i] = ld128(ap[(st) >> 1] + ((st) & 1) * M_GS + i * 1024); BC[i] = ldtr(bp[(st) >> 1] + ((st) & 1) * M_GS + i * 2048); SBAR(); \
                    acc = MFMA32(BP[i], AP[i], acc); PIN(acc); SBAR(); } \
                } while (0)
            LGKM0();
            for (int it = 0; it < 4; ++it) { F_ITER(0, a1, b1, a0, b0); F_ITER(1, a0, b0, a1, b1); F_ITER(2, a1, b1, a0, b0); F_ITER(3, a0, b0, a1, b1); }
#pragma unroll
            for (int i = 0; i < 4; ++i) acc = MFMA32(b1[i], a1[i], acc);
#pragma unroll
            for (int s = 0; s < 2; ++s) {
                const u32x4 ya = *(const LAS u32x4*)(lp + w * 2048 + s * 1024), yb = *(const LAS u32x4*)(lp + 16384 + w * 2048 + s * 1024);
#pragma unroll
                for (int i = 0; i < 4; ++i) {
                    acc[8 * s + 2 * i] += __uint_as_float(ya[i] << 16) + __uint_as_float(yb[i] << 16);
                    acc[8 * s + 2 * i + 1] += __uint_as_float(ya[i] & 0xffff0000u) + __uint_as_float(yb[i] & 0xffff0000u);
                }
            }
            u16* op = out + (size_t)(tt * 128 + wm * 32 + r) * 1024 + (2 * et + part) * 64 + wn * 32 + hh * 8;
#pragma unroll
            for (int s = 0; s < 2; ++s) ST16(op + s * 16, pack8(acc, s, 1.f));
        }
        if (blockIdx.x == 0 && w == 0 && lane == 0) st_tag(ws + WS_E, tag);
        __builtin_amdgcn_s_waitcnt(0x0F70);
    }
}

extern "C" void kernel_launch(void* const* d_in, const int* in_sizes, int n_in, void* d_out, int out_size, void* d_ws, size_t ws_size,
                              hipStream_t stream) {
    (void)hipFuncSetAttribute((const void*)k_all, hipFuncAttributeMaxDynamicSharedMemorySize, M_LDS);
    k_all<<<256, 1024, M_LDS, stream>>>((const u16*)d_in[0], (const u16*)d_in[1], (const u16*)d_in[2], (const u16*)d_in[3], (const u16*)d_in[4],
                                        (const u16*)d_in[6], (const u16*)d_in[7], (const u16*)d_in[8], (char*)d_ws, (u16*)d_out);
}
```
